```python
import torch

def _softmax(x, axis=-1):
    return torch.softmax(x, dim=axis)
def _gelu(x):
    return torch.nn.functional.gelu(x, approximate="tanh")
def _silu(x):
    return torch.nn.functional.silu(x)
def _sigmoid(x):
    return torch.sigmoid(x)
def _layernorm(x, axis=-1, eps=1e-5):
    m = x.mean(dim=axis, keepdim=True)
    v = x.var(dim=axis, keepdim=True, unbiased=False)
    return (x - m) / torch.sqrt(v + eps)
def _scatter_add(tgt, idx, src, axis=0):
    return tgt.clone().index_add_(axis, idx, src)
def _segment_sum(src, seg, n):
    out = torch.zeros((n,) + src.shape[1:], dtype=src.dtype)
    return out.index_add_(0, seg, src)
def _pad(x, pw):
    flat = tuple(v for pair in reversed(pw) for v in pair)
    return torch.nn.functional.pad(x, flat)

kernel_name = 'standalone__standard_2d_1d_3d_conv__vision'

def setup_inputs():

    _w = (lambda a: (lambda t: t.to(torch.bfloat16) if t.is_floating_point() else t)(torch.as_tensor(a)))
    H = W = 384
    C = 192
    K = 1
    return {
        "x": _w((lambda *s: torch.randn(*s, dtype=torch.bfloat16) * 0.05)(1, H, W, C)),
        "w": _w((lambda *s: torch.randn(*s, dtype=torch.bfloat16) * 0.05)(K, K, C, C)),
        "b": _w((lambda *s: torch.randn(*s, dtype=torch.bfloat16) * 0.05)(C)),
    }

def reference(x, w, b):
    B, H, W, Cin = x.shape
    K = w.shape[0]
    pl = (K - 1) // 2
    pr = K // 2
    xp = _pad(x, ((0, 0), (pl, pr), (pl, pr), (0, 0)))
    out = sum(
        torch.einsum('bhwc,cd->bhwd', xp[:, i:i+H, j:j+W, :], w[i, j])
        for i in range(K) for j in range(K)
    )
    return out + b[None, None, None, :]
```

```cpp
#include <hip/hip_runtime.h>
#include <hip/hip_bf16.h>
#include <stdint.h>

typedef __attribute__((ext_vector_type(8))) __bf16 bf16x8;
typedef __attribute__((ext_vector_type(4))) float f32x4;
typedef __attribute__((ext_vector_type(4))) unsigned int u32x4;
typedef __attribute__((ext_vector_type(2))) unsigned int u32x2;

constexpr int C = 192;

__device__ __forceinline__ unsigned pack_bf16(float lo, float hi) {
    typedef __attribute__((ext_vector_type(2))) __bf16 bf16x2;
    bf16x2 v; v[0] = (__bf16)lo; v[1] = (__bf16)hi;
    return __builtin_bit_cast(unsigned, v);
}

__global__ __launch_bounds__(256) void conv1x1_kernel(const __hip_bfloat16* __restrict__ x,
                                                      const __hip_bfloat16* __restrict__ w,
                                                      const __hip_bfloat16* __restrict__ bias,
                                                      __hip_bfloat16* __restrict__ out, int M) {
    __shared__ __attribute__((aligned(16))) unsigned short wl[72 * 64 * 8];
    const int tid = threadIdx.x;
    const int lane = tid & 63, wid = tid >> 6;
    const unsigned short* wg = (const unsigned short*)w;
    for (int idx = tid; idx < 72 * 64; idx += 256) {
        int f = idx >> 6, l = idx & 63;
        int s = f / 12, t = f % 12;
        int c = l & 15, q = l >> 4;
        int u = t >> 1, e = t & 1, p = c >> 2, r = c & 3;
        int ch = 32 * u + 8 * p + 4 * e + r;
        for (int j = 0; j < 8; ++j) wl[idx * 8 + j] = wg[(32 * s + 8 * q + j) * C + ch];
    }
    __syncthreads();
    const int c = lane & 15, q = lane >> 4;
    float bv[6][8];
    for (int u = 0; u < 6; ++u)
        for (int j = 0; j < 8; ++j) bv[u][j] = __bfloat162float(bias[32 * u + 8 * q + j]);
    const int ntiles = M / 16;
    for (int tile = blockIdx.x * 4 + wid; tile < ntiles; tile += gridDim.x * 4) {
        const __hip_bfloat16* xr = x + (size_t)(tile * 16 + c) * C + 8 * q;
        bf16x8 xf[6];
#pragma unroll
        for (int s = 0; s < 6; ++s) xf[s] = *(const bf16x8*)(xr + 32 * s);
        f32x4 acc[12];
#pragma unroll
        for (int t = 0; t < 12; ++t) acc[t] = f32x4{0.f, 0.f, 0.f, 0.f};
#pragma unroll
        for (int s = 0; s < 6; ++s)
#pragma unroll
            for (int t = 0; t < 12; ++t) {
                bf16x8 wf = *(const bf16x8*)(wl + ((s * 12 + t) * 64 + lane) * 8);
                acc[t] = __builtin_amdgcn_mfma_f32_16x16x32_bf16(wf, xf[s], acc[t], 0, 0, 0);
            }
        __hip_bfloat16* orow = out + (size_t)(tile * 16 + c) * C + 8 * q;
#pragma unroll
        for (int u = 0; u < 6; ++u) {
            float v[8];
#pragma unroll
            for (int j = 0; j < 8; ++j) {
                float a = acc[2 * u + (j >> 2)][j & 3];
                a = (float)(__bf16)a;
                v[j] = a + bv[u][j];
            }
            u32x4 o;
            o[0] = pack_bf16(v[0], v[1]); o[1] = pack_bf16(v[2], v[3]);
            o[2] = pack_bf16(v[4], v[5]); o[3] = pack_bf16(v[6], v[7]);
            *(u32x4*)(orow + 32 * u) = o;
        }
    }
}

extern "C" void kernel_launch(void* const* d_in, const int* in_sizes, int n_in,
                              void* d_out, int out_size, void* d_ws, size_t ws_size,
                              hipStream_t stream) {
    const __hip_bfloat16* x = (const __hip_bfloat16*)d_in[0];
    const __hip_bfloat16* w = (const __hip_bfloat16*)d_in[1];
    const __hip_bfloat16* b = (const __hip_bfloat16*)d_in[2];
    __hip_bfloat16* out = (__hip_bfloat16*)d_out;
    int M = in_sizes[0] / C;
    conv1x1_kernel<<<512, 256, 0, stream>>>(x, w, b, out, M);
}
```

```cpp
#include <hip/hip_runtime.h>
#include <hip/hip_bf16.h>
#include <stdint.h>

#define LNT 1
#define SA 16
#define CODEPF 1

typedef __attribute__((ext_vector_type(2))) __bf16 bf16x2;
typedef __attribute__((ext_vector_type(4))) float f32x4;
typedef __attribute__((ext_vector_type(4))) unsigned int u32x4;
typedef __attribute__((ext_vector_type(2))) unsigned int u32x2;
typedef __attribute__((ext_vector_type(4))) short s16x4;
typedef __attribute__((ext_vector_type(8))) short s16x8;
#define LDSP __attribute__((address_space(3)))

constexpr int C = 192;
constexpr int NW = 4;
constexpr int TPW = 9;
constexpr int ROWB = C * 2;
constexpr int SLOT = 16 * ROWB;
constexpr int JSTR = NW * SLOT;
constexpr int W_OFF = 3 * JSTR;
constexpr int BIAS_OFF = 6 * JSTR;
constexpr int LDS_BYTES = BIAS_OFF + 1024;

#define STAMP(k)

#define WAIT_V(n) asm volatile("s_waitcnt vmcnt(" #n ")" ::: "memory")
#define WAIT_L0() asm volatile("s_waitcnt lgkmcnt(0)" ::: "memory")
#define BAR() __builtin_amdgcn_s_barrier()
#define SCHED() __builtin_amdgcn_sched_barrier(0)

__device__ __forceinline__ unsigned pack_bf16(float lo, float hi) {
    bf16x2 v; v[0] = (__bf16)lo; v[1] = (__bf16)hi;
    return __builtin_bit_cast(unsigned, v);
}

template <bool NT>
__device__ __forceinline__ void dma(unsigned m0v, unsigned voff, __amdgpu_buffer_rsrc_t r, unsigned soff) {
    if (NT) asm volatile("s_mov_b32 m0, %0\n\ts_nop 0\n\tbuffer_load_dwordx4 %1, %2, %3 offen nt lds" :: "s"(m0v), "v"(voff), "s"(r), "s"(soff) : "memory");
    else    asm volatile("s_mov_b32 m0, %0\n\ts_nop 0\n\tbuffer_load_dwordx4 %1, %2, %3 offen lds" :: "s"(m0v), "v"(voff), "s"(r), "s"(soff) : "memory");
}

template <bool WA>
__device__ __forceinline__ void mfma_first(f32x4& d, const s16x8& wv, const s16x8& xv, const f32x4& cv) {
    if (WA) asm volatile("v_mfma_f32_16x16x32_bf16 %0, %1, %2, %3" : "=&v"(d) : "a"(wv), "v"(xv), "v"(cv));
    else    asm volatile("v_mfma_f32_16x16x32_bf16 %0, %1, %2, %3" : "=&v"(d) : "v"(wv), "v"(xv), "v"(cv));
}
template <bool WA>
__device__ __forceinline__ void mfma_acc(f32x4& d, const s16x8& wv, const s16x8& xv) {
    if (WA) asm volatile("v_mfma_f32_16x16x32_bf16 %0, %1, %2, %0" : "+v"(d) : "a"(wv), "v"(xv));
    else    asm volatile("v_mfma_f32_16x16x32_bf16 %0, %1, %2, %0" : "+v"(d) : "v"(wv), "v"(xv));
}

__global__ __launch_bounds__(NW * 64, 1) void conv1x1_kernel(const void* __restrict__ x,
                                                             const void* __restrict__ w,
                                                             const void* __restrict__ bias,
                                                             void* __restrict__ out, int nbytes
                                                             ) {
    __shared__ __attribute__((aligned(16))) char lds[LDS_BYTES];
    STAMP(0);
    const int tid = threadIdx.x;
    const unsigned lane = tid & 63;
    const unsigned wid = __builtin_amdgcn_readfirstlane(tid >> 6);
    const unsigned lbase = (unsigned)(size_t)(LDSP char*)lds;

    auto rx = __builtin_amdgcn_make_buffer_rsrc((void*)x, 0, nbytes, 0x00020000);
    auto ro = __builtin_amdgcn_make_buffer_rsrc(out, 0, nbytes, 0x00020000);
    auto rw = __builtin_amdgcn_make_buffer_rsrc((void*)w, 0, C * C * 2, 0x00020000);
    auto rb = __builtin_amdgcn_make_buffer_rsrc((void*)bias, 0, C * 2, 0x00020000);

    unsigned xo[6], wo[6];
#pragma unroll
    for (int u = 0; u < 6; ++u) {
        unsigned line = 8 * u + (lane >> 3), row = line / 3;
        xo[u] = line * 128 + (((lane & 7) ^ ((row >> 1) & 7)) << 4);
        wo[u] = line * 128 + (((lane & 7) ^ ((row & 2) | ((row >> 1) & 4))) << 4);
    }
    const unsigned tstride = gridDim.x * NW * SLOT;
    const unsigned t0 = (blockIdx.x * NW + wid) * SLOT;
    const unsigned sl0 = lbase + wid * SLOT;

    dma<false>(lbase + BIAS_OFF, lane * 16, rb, 0);
#pragma unroll 1
    for (unsigned j = 0; j < 3; ++j) {
#pragma unroll
        for (int u = 0; u < 6; ++u) {
            dma<LNT>(sl0 + j * JSTR + u * 1024, xo[u], rx, t0 + j * tstride);
            dma<false>(sl0 + W_OFF + j * JSTR + u * 1024, wo[u], rw, j * JSTR + wid * SLOT);
        }
    }
    STAMP(1);
    WAIT_V(12);
    BAR();
    STAMP(2);

    s16x8 wf[6][12];
    unsigned wa[4];
    {
        unsigned g = lane >> 4, r = (lane >> 2) & 3, p = lane & 3;
        unsigned fw = (r & 2) | ((g & 1) << 2);
#pragma unroll
        for (int v = 0; v < 4; ++v)
            wa[v] = lbase + W_OFF + (8 * g + r) * ROWB + 8 * (p & 1) + ((((2 * v) | (p >> 1)) ^ fw) << 4);
    }
    auto read_w = [&](int s) {
#pragma unroll
        for (int t = 0; t < 12; ++t) {
            const LDSP char* a = (const LDSP char*)(size_t)wa[t & 3] + s * 32 * ROWB + (t >> 2) * 128;
            s16x4 lo = __builtin_amdgcn_ds_read_tr16_b64_v4i16((LDSP s16x4*)a);
            s16x4 hi = __builtin_amdgcn_ds_read_tr16_b64_v4i16((LDSP s16x4*)(a + 4 * ROWB));
            wf[s][t] = __builtin_shufflevector(lo, hi, 0, 1, 2, 3, 4, 5, 6, 7);
        }
    };
    const unsigned c = lane & 15, q = lane >> 4;
    u32x2 braw[12];
#pragma unroll
    for (int t = 0; t < 12; ++t) braw[t] = *(const LDSP u32x2*)((LDSP char*)lds + BIAS_OFF + 32 * t + 8 * q);
    read_w(0); read_w(1); read_w(2); read_w(3);
    f32x4 bv[12];
#pragma unroll
    for (int t = 0; t < 12; ++t) {
        bv[t][0] = __builtin_bit_cast(float, braw[t][0] << 16);
        bv[t][1] = __builtin_bit_cast(float, braw[t][0] & 0xffff0000u);
        bv[t][2] = __builtin_bit_cast(float, braw[t][1] << 16);
        bv[t][3] = __builtin_bit_cast(float, braw[t][1] & 0xffff0000u);
    }
    SCHED();
    WAIT_L0();
    BAR();
#pragma unroll 1
    for (unsigned j = 3; j < 5; ++j) {
#pragma unroll
        for (int u = 0; u < 6; ++u) dma<LNT>(sl0 + j * JSTR + u * 1024, xo[u], rx, t0 + j * tstride);
    }
    WAIT_V(12);
    BAR();
    read_w(4); read_w(5);

    const unsigned f = (c >> 1) & 7;
    const unsigned xa0 = c * ROWB + ((q ^ f) << 4), xa1 = c * ROWB + (((q | 4) ^ f) << 4);
    unsigned oa[4];
#pragma unroll
    for (int v = 0; v < 4; ++v) oa[v] = c * ROWB + ((((2 * v) | (q >> 1)) ^ f) << 4) + 8 * (q & 1);

    s16x8 xf[6];
#pragma unroll
    for (int s = 0; s < 6; ++s) xf[s] = *(const LDSP s16x8*)((LDSP char*)(size_t)sl0 + ((s & 1) ? xa1 : xa0) + (s >> 1) * 128);
    SCHED();
    WAIT_L0();
    BAR();
    STAMP(3);

    f32x4 acc[2][2];
    acc[1][0] = bv[0]; acc[1][1] = bv[0];
    u32x4 ov[6];
    unsigned slot = sl0, pslot = sl0 + 5 * JSTR, gofs = t0;

    auto epi = [&](int g, unsigned sl, int k) {
        const int t = 2 * g + k;
        u32x2 o; o[0] = pack_bf16(acc[g & 1][k][0], acc[g & 1][k][1]); o[1] = pack_bf16(acc[g & 1][k][2], acc[g & 1][k][3]);
        *(LDSP u32x2*)((LDSP char*)(size_t)sl + oa[t & 3] + (t >> 2) * 128) = o;
    };

#pragma unroll 1
    for (int i = 0; i < TPW; ++i) {
        const unsigned nslot = (i == 5) ? sl0 : slot + JSTR;
#pragma unroll
        for (int g = 0; g < 6; ++g) {
#pragma unroll
            for (int s = 0; s < 6; ++s) {
#pragma unroll
                for (int k = 0; k < 2; ++k) {
                    const int t = 2 * g + k;
                    if (s == 0) { if (s * 12 + t < 64) mfma_first<true>(acc[g & 1][k], wf[s][t], xf[s], bv[t]); else mfma_first<false>(acc[g & 1][k], wf[s][t], xf[s], bv[t]); }
                    else        { if (s * 12 + t < 64) mfma_acc<true>(acc[g & 1][k], wf[s][t], xf[s]); else mfma_acc<false>(acc[g & 1][k], wf[s][t], xf[s]); }
                    SCHED();
                }
                if (g == 1 && s < 3 && true) {
                    ov[2 * s]     = *(const LDSP u32x4*)((LDSP char*)(size_t)pslot + lane * 16 + (2 * s) * 1024);
                    ov[2 * s + 1] = *(const LDSP u32x4*)((LDSP char*)(size_t)pslot + lane * 16 + (2 * s + 1) * 1024);
                }
                if (g == 2 && s == 0) {
                    if (i < TPW - 5) {
                        WAIT_L0();
#pragma unroll
                        for (int u = 0; u < 6; ++u) dma<LNT>(pslot + u * 1024, xo[u], rx, gofs + 5 * tstride);
                    }
                }
                if (g == 3 && s == 0) {
                    if (i > 0) {
#pragma unroll
                        for (int u = 0; u < 6; ++u) __builtin_amdgcn_raw_buffer_store_b128(ov[u], ro, xo[u], gofs - tstride, SA);
                    }
                }
                if (s == 4 || s == 5) {
                    if (g == 0) { if (i > 0) epi(5, pslot, s - 4); }
                    else epi(g - 1, slot, s - 4);
                }
                if (g == 4 && s == 5) WAIT_V(30);
                if (g == 5) xf[s] = *(const LDSP s16x8*)((LDSP char*)(size_t)nslot + ((s & 1) ? xa1 : xa0) + (s >> 1) * 128);
                SCHED();
            }
        }
        pslot = slot; slot = nslot; gofs += tstride;
    }
    asm volatile("s_nop 7" ::: "memory");
    SCHED();
    epi(5, pslot, 0); epi(5, pslot, 1);
#pragma unroll
    for (int u = 0; u < 6; ++u) ov[u] = *(const LDSP u32x4*)((LDSP char*)(size_t)pslot + lane * 16 + u * 1024);
#pragma unroll
    for (int u = 0; u < 6; ++u) __builtin_amdgcn_raw_buffer_store_b128(ov[u], ro, xo[u], gofs - tstride, SA);
}

extern "C" void kernel_launch(void* const* d_in, const int* in_sizes, int n_in,
                              void* d_out, int out_size, void* d_ws, size_t ws_size,
                              hipStream_t stream) {
    int M = in_sizes[0] / C;
    int grid = M / (16 * NW * TPW);
    conv1x1_kernel<<<grid, NW * 64, 0, stream>>>(d_in[0], d_in[1], d_in[2], d_out, M * ROWB
    );
}
```

```cpp
#include <hip/hip_runtime.h>
#include <hip/hip_bf16.h>
#include <stdint.h>

#define LNT 1
#define SA 2
#define TMAP 0
#define CODEPF 1

typedef __attribute__((ext_vector_type(2))) __bf16 bf16x2;
typedef __attribute__((ext_vector_type(4))) float f32x4;
typedef __attribute__((ext_vector_type(4))) unsigned int u32x4;
typedef __attribute__((ext_vector_type(2))) unsigned int u32x2;
typedef __attribute__((ext_vector_type(4))) short s16x4;
typedef __attribute__((ext_vector_type(8))) short s16x8;
#define LDSP __attribute__((address_space(3)))

constexpr int C = 192;
constexpr int NW = 4;
constexpr int TPW = 9;
constexpr int ROWB = C * 2;
constexpr int SLOT = 16 * ROWB;
constexpr int JSTR = NW * SLOT;
constexpr int W_OFF = 3 * JSTR;
constexpr int BIAS_OFF = 6 * JSTR;
constexpr int LDS_BYTES = BIAS_OFF + 1024 + 256;

#define STAMP(k)

#define WAIT_V(n) asm volatile("s_waitcnt vmcnt(" #n ")" ::: "memory")
#define WAIT_L0() asm volatile("s_waitcnt lgkmcnt(0)" ::: "memory")
#define BAR() __builtin_amdgcn_s_barrier()
#define SCHED() __builtin_amdgcn_sched_barrier(0)

__device__ __forceinline__ unsigned pack_bf16(float lo, float hi) {
    bf16x2 v; v[0] = (__bf16)lo; v[1] = (__bf16)hi;
    return __builtin_bit_cast(unsigned, v);
}

template <bool NT>
__device__ __forceinline__ void dma(unsigned m0v, unsigned voff, __amdgpu_buffer_rsrc_t r, unsigned soff) {
    if (NT) asm volatile("s_mov_b32 m0, %0\n\ts_nop 0\n\tbuffer_load_dwordx4 %1, %2, %3 offen nt lds" :: "s"(m0v), "v"(voff), "s"(r), "s"(soff) : "memory");
    else    asm volatile("s_mov_b32 m0, %0\n\ts_nop 0\n\tbuffer_load_dwordx4 %1, %2, %3 offen lds" :: "s"(m0v), "v"(voff), "s"(r), "s"(soff) : "memory");
}

template <bool WA>
__device__ __forceinline__ void mfma_first(f32x4& d, const s16x8& wv, const s16x8& xv, const f32x4& cv) {
    if (WA) asm volatile("v_mfma_f32_16x16x32_bf16 %0, %1, %2, %3" : "=&v"(d) : "a"(wv), "v"(xv), "v"(cv));
    else    asm volatile("v_mfma_f32_16x16x32_bf16 %0, %1, %2, %3" : "=&v"(d) : "v"(wv), "v"(xv), "v"(cv));
}
template <bool WA>
__device__ __forceinline__ void mfma_acc(f32x4& d, const s16x8& wv, const s16x8& xv) {
    if (WA) asm volatile("v_mfma_f32_16x16x32_bf16 %0, %1, %2, %0" : "+v"(d) : "a"(wv), "v"(xv));
    else    asm volatile("v_mfma_f32_16x16x32_bf16 %0, %1, %2, %0" : "+v"(d) : "v"(wv), "v"(xv));
}

__global__ __launch_bounds__(NW * 64, 1) void conv1x1_kernel(const void* __restrict__ x,
                                                             const void* __restrict__ w,
                                                             const void* __restrict__ bias,
                                                             void* __restrict__ out, int nbytes
                                                             ) {
    __shared__ __attribute__((aligned(16))) char lds[LDS_BYTES];
    STAMP(0);
    const int tid = threadIdx.x;
    const unsigned lane = tid & 63;
    const unsigned wid = __builtin_amdgcn_readfirstlane(tid >> 6);
    const unsigned lbase = (unsigned)(size_t)(LDSP char*)lds;

    {
        unsigned long long pc; unsigned clen;
        asm volatile("s_getpc_b64 %0\n.Lconv_pc:\n\ts_mov_b32 %1, .Lconv_end-.Lconv_pc" : "=s"(pc), "=s"(clen));
        auto rc = __builtin_amdgcn_make_buffer_rsrc((void*)pc, 0, clen, 0x00020000);
        asm volatile("s_mov_b32 m0, %0\n\ts_nop 0\n\tbuffer_load_dword %1, %2, 0 offen lds" :: "s"(lbase + BIAS_OFF + 1024), "v"(lane * 128), "s"(rc) : "memory");
    }
    auto rx = __builtin_amdgcn_make_buffer_rsrc((void*)x, 0, nbytes, 0x00020000);
    auto ro = __builtin_amdgcn_make_buffer_rsrc(out, 0, nbytes, 0x00020000);
    auto rw = __builtin_amdgcn_make_buffer_rsrc((void*)w, 0, C * C * 2, 0x00020000);
    auto rb = __builtin_amdgcn_make_buffer_rsrc((void*)bias, 0, C * 2, 0x00020000);

    unsigned xo[6], wo[6];
#pragma unroll
    for (int u = 0; u < 6; ++u) {
        unsigned line = 8 * u + (lane >> 3), row = line / 3;
        xo[u] = line * 128 + (((lane & 7) ^ ((row >> 1) & 7)) << 4);
        wo[u] = line * 128 + (((lane & 7) ^ ((row & 2) | ((row >> 1) & 4))) << 4);
    }
    const unsigned tstride = gridDim.x * NW * SLOT;
    const unsigned t0 = (blockIdx.x * NW + wid) * SLOT;
    const unsigned sl0 = lbase + wid * SLOT;

    dma<false>(lbase + BIAS_OFF, lane * 16, rb, 0);
#pragma unroll 1
    for (unsigned j = 0; j < 3; ++j) {
#pragma unroll
        for (int u = 0; u < 6; ++u) {
            dma<LNT>(sl0 + j * JSTR + u * 1024, xo[u], rx, t0 + j * tstride);
            dma<false>(sl0 + W_OFF + j * JSTR + u * 1024, wo[u], rw, j * JSTR + wid * SLOT);
        }
    }
    STAMP(1);
    WAIT_V(12);
    BAR();
    STAMP(2);

    s16x8 wf[6][12];
    unsigned wa[4];
    {
        unsigned g = lane >> 4, r = (lane >> 2) & 3, p = lane & 3;
        unsigned fw = (r & 2) | ((g & 1) << 2);
#pragma unroll
        for (int v = 0; v < 4; ++v)
            wa[v] = lbase + W_OFF + (8 * g + r) * ROWB + 8 * (p & 1) + ((((2 * v) | (p >> 1)) ^ fw) << 4);
    }
    auto read_w = [&](int s) {
#pragma unroll
        for (int t = 0; t < 12; ++t) {
            const LDSP char* a = (const LDSP char*)(size_t)wa[t & 3] + s * 32 * ROWB + (t >> 2) * 128;
            s16x4 lo = __builtin_amdgcn_ds_read_tr16_b64_v4i16((LDSP s16x4*)a);
            s16x4 hi = __builtin_amdgcn_ds_read_tr16_b64_v4i16((LDSP s16x4*)(a + 4 * ROWB));
            wf[s][t] = __builtin_shufflevector(lo, hi, 0, 1, 2, 3, 4, 5, 6, 7);
        }
    };
    const unsigned c = lane & 15, q = lane >> 4;
    u32x2 braw[12];
#pragma unroll
    for (int t = 0; t < 12; ++t) braw[t] = *(const LDSP u32x2*)((LDSP char*)lds + BIAS_OFF + 32 * t + 8 * q);
    read_w(0); read_w(1); read_w(2); read_w(3);
    f32x4 bv[12];
#pragma unroll
    for (int t = 0; t < 12; ++t) {
        bv[t][0] = __builtin_bit_cast(float, braw[t][0] << 16);
        bv[t][1] = __builtin_bit_cast(float, braw[t][0] & 0xffff0000u);
        bv[t][2] = __builtin_bit_cast(float, braw[t][1] << 16);
        bv[t][3] = __builtin_bit_cast(float, braw[t][1] & 0xffff0000u);
    }
    SCHED();
    WAIT_L0();
    BAR();
#pragma unroll 1
    for (unsigned j = 3; j < 5; ++j) {
#pragma unroll
        for (int u = 0; u < 6; ++u) dma<LNT>(sl0 + j * JSTR + u * 1024, xo[u], rx, t0 + j * tstride);
    }
    WAIT_V(12);
    BAR();
    read_w(4); read_w(5);

    const unsigned f = (c >> 1) & 7;
    const unsigned xa0 = c * ROWB + ((q ^ f) << 4), xa1 = c * ROWB + (((q | 4) ^ f) << 4);
    unsigned oa[4];
#pragma unroll
    for (int v = 0; v < 4; ++v) oa[v] = c * ROWB + ((((2 * v) | (q >> 1)) ^ f) << 4) + 8 * (q & 1);

    s16x8 xf[6];
#pragma unroll
    for (int s = 0; s < 6; ++s) xf[s] = *(const LDSP s16x8*)((LDSP char*)(size_t)sl0 + ((s & 1) ? xa1 : xa0) + (s >> 1) * 128);
    SCHED();
    WAIT_L0();
    BAR();
    STAMP(3);

    f32x4 acc[2][2];
    acc[1][0] = bv[0]; acc[1][1] = bv[0];
    u32x4 ov[6];
    unsigned slot = sl0, pslot = sl0 + 5 * JSTR, gofs = t0;

    auto epi = [&](int g, unsigned sl, int k) {
        const int t = 2 * g + k;
        u32x2 o; o[0] = pack_bf16(acc[g & 1][k][0], acc[g & 1][k][1]); o[1] = pack_bf16(acc[g & 1][k][2], acc[g & 1][k][3]);
        *(LDSP u32x2*)((LDSP char*)(size_t)sl + oa[t & 3] + (t >> 2) * 128) = o;
    };

#pragma unroll 1
    for (int i = 0; i < TPW; ++i) {
        const unsigned nslot = (i == 5) ? sl0 : slot + JSTR;
#pragma unroll
        for (int g = 0; g < 6; ++g) {
#pragma unroll
            for (int s = 0; s < 6; ++s) {
#pragma unroll
                for (int k = 0; k < 2; ++k) {
                    const int t = 2 * g + k;
                    if (s == 0) { if (s * 12 + t < 64) mfma_first<true>(acc[g & 1][k], wf[s][t], xf[s], bv[t]); else mfma_first<false>(acc[g & 1][k], wf[s][t], xf[s], bv[t]); }
                    else        { if (s * 12 + t < 64) mfma_acc<true>(acc[g & 1][k], wf[s][t], xf[s]); else mfma_acc<false>(acc[g & 1][k], wf[s][t], xf[s]); }
                    SCHED();
                }
                if (g == 1 && s < 3 && true) {
                    ov[2 * s]     = *(const LDSP u32x4*)((LDSP char*)(size_t)pslot + lane * 16 + (2 * s) * 1024);
                    ov[2 * s + 1] = *(const LDSP u32x4*)((LDSP char*)(size_t)pslot + lane * 16 + (2 * s + 1) * 1024);
                }
                if (g == 2 && s == 0) {
                    if (i < TPW - 5) {
                        WAIT_L0();
#pragma unroll
                        for (int u = 0; u < 6; ++u) dma<LNT>(pslot + u * 1024, xo[u], rx, gofs + 5 * tstride);
                    }
                }
                if (g == 3 && s == 0) {
                    if (i > 0) {
#pragma unroll
                        for (int u = 0; u < 6; ++u) __builtin_amdgcn_raw_buffer_store_b128(ov[u], ro, xo[u], gofs - tstride, SA);
                    }
                }
                if (s == 4 || s == 5) {
                    if (g == 0) { if (i > 0) epi(5, pslot, s - 4); }
                    else epi(g - 1, slot, s - 4);
                }
                if (g == 4 && s == 5) WAIT_V(30);
                if (g == 5) xf[s] = *(const LDSP s16x8*)((LDSP char*)(size_t)nslot + ((s & 1) ? xa1 : xa0) + (s >> 1) * 128);
                SCHED();
            }
        }
        pslot = slot; slot = nslot; gofs += tstride;
    }
    asm volatile("s_nop 7" ::: "memory");
    SCHED();
    epi(5, pslot, 0); epi(5, pslot, 1);
#pragma unroll
    for (int u = 0; u < 6; ++u) ov[u] = *(const LDSP u32x4*)((LDSP char*)(size_t)pslot + lane * 16 + u * 1024);
#pragma unroll
    for (int u = 0; u < 6; ++u) __builtin_amdgcn_raw_buffer_store_b128(ov[u], ro, xo[u], gofs - tstride, SA);
    asm volatile(".Lconv_end:");
}

extern "C" void kernel_launch(void* const* d_in, const int* in_sizes, int n_in,
                              void* d_out, int out_size, void* d_ws, size_t ws_size,
                              hipStream_t stream) {
    int M = in_sizes[0] / C;
    int grid = M / (16 * NW * TPW);
    conv1x1_kernel<<<grid, NW * 64, 0, stream>>>(d_in[0], d_in[1], d_in[2], d_out, M * ROWB
    );
}
```

```cpp
#include <hip/hip_runtime.h>
#include <hip/hip_bf16.h>
#include <stdint.h>

#define LDSP __attribute__((address_space(3)))

constexpr int C = 192;
constexpr int NW = 4;
constexpr int TPW = 9;
constexpr int ROWB = C * 2;
constexpr int SLOT = 16 * ROWB;
constexpr int JSTR = NW * SLOT;
constexpr int W_OFF = 3 * JSTR;
constexpr int BIAS_OFF = 6 * JSTR;
constexpr int SINK_OFF = BIAS_OFF + 1024;
constexpr int STG_OFF = SINK_OFF + 256;
constexpr int LDS_BYTES = STG_OFF + NW * 2048;
constexpr unsigned OOR = 0x7fffff00u;

__global__ __launch_bounds__(NW * 64, 1) void conv1x1_kernel(const void* __restrict__ x, const void* __restrict__ w,
                                                             const void* __restrict__ bias, void* __restrict__ out,
                                                             unsigned nbytes, unsigned tstride) {
    __shared__ __attribute__((aligned(128))) char lds[LDS_BYTES];
    const int tid = threadIdx.x;
    const unsigned lane = tid & 63;
    const unsigned wid = __builtin_amdgcn_readfirstlane(tid >> 6);
    const unsigned lbase = (unsigned)(size_t)(LDSP char*)lds;
    const unsigned share = ((blockIdx.x >> 3) * NW + wid) & 31;

    {
        unsigned long long pc; unsigned clen;
        asm volatile("s_getpc_b64 %0\n.Lconv_pc:\n\ts_mov_b32 %1, .Lconv_end-.Lconv_pc" : "=s"(pc), "=s"(clen));
        auto rc = __builtin_amdgcn_make_buffer_rsrc((void*)pc, 0, clen, 0x00020000);
        asm volatile("s_mov_b32 m0, %0\n\ts_nop 0\n\tbuffer_load_dword %1, %2, 0 offen lds"
                     :: "s"(lbase + SINK_OFF), "v"(lane < 4 ? (share * 4 + lane) * 128 : OOR), "s"(rc) : "memory");
    }
    auto rx = __builtin_amdgcn_make_buffer_rsrc((void*)x, 0, nbytes, 0x00020000);
    auto rw = __builtin_amdgcn_make_buffer_rsrc((void*)w, 0, C * C * 2, 0x00020000);
    auto rb = __builtin_amdgcn_make_buffer_rsrc((void*)bias, 0, C * 2, 0x00020000);

    unsigned xo[6], wo[6];
#pragma unroll
    for (int u = 0; u < 3; ++u) {
        const unsigned d6 = ((8 * u + (lane >> 3)) * 43) >> 8;
        xo[u] = (lane * 16) ^ (d6 << 4);
        wo[u] = (lane * 16) ^ ((d6 & 1) << 5);
        xo[u + 3] = xo[u] ^ (3072 + 64);
        wo[u + 3] = wo[u] ^ (3072 + 64);
    }
    const unsigned t0 = (blockIdx.x * NW + wid) * SLOT;
    const unsigned sl0 = lbase + wid * SLOT;

    asm volatile("s_mov_b32 m0, %0\n\ts_nop 0\n\tbuffer_load_dword %1, %2, %3 offen lds"
                 :: "s"(lbase + SINK_OFF), "v"(lane < 18 ? lane * 128 : OOR), "s"(rw), "s"(share * 2304) : "memory");
    asm volatile("s_mov_b32 m0, %0\n\ts_nop 0\n\tbuffer_load_dwordx4 %1, %2, 0 offen lds"
                 :: "s"(lbase + BIAS_OFF), "v"(lane * 16), "s"(rb) : "memory");

    const unsigned olo = (unsigned)(size_t)out, ohi = (unsigned)((size_t)out >> 32) & 0xffffu;

    asm volatile(
        "s_memrealtime s[72:73]\n\t"
        "s_mov_b32 s64, %[t0]\n\t"
        "s_mov_b32 s65, %[ws]\n\t"
        "s_mov_b32 s67, %[t0]\n\t"
        "s_mov_b32 s68, 0\n\t"
        "s_add_u32 m0, %[sl0], 73728\n\t"
        "s_nop 0\n\t"
        "buffer_load_dwordx4 %[wo0], %[rw], s65 offen lds\n\t"
        "buffer_load_dwordx4 %[wo1], %[rw], s65 offen offset:1024 lds\n\t"
        "buffer_load_dwordx4 %[wo2], %[rw], s65 offen offset:2048 lds\n\t"
        "s_add_u32 m0, %[sl0], 0\n\t"
        "s_nop 0\n\t"
        "buffer_load_dwordx4 %[xo0], %[rx], s64 offen nt lds\n\t"
        "buffer_load_dwordx4 %[xo1], %[rx], s64 offen offset:1024 nt lds\n\t"
        "buffer_load_dwordx4 %[xo2], %[rx], s64 offen offset:2048 nt lds\n\t"
        "s_add_u32 m0, %[sl0], 3072\n\t"
        "s_nop 0\n\t"
        "buffer_load_dwordx4 %[xo3], %[rx], s64 offen nt lds\n\t"
        "buffer_load_dwordx4 %[xo4], %[rx], s64 offen offset:1024 nt lds\n\t"
        "buffer_load_dwordx4 %[xo5], %[rx], s64 offen offset:2048 nt lds\n\t"
        "s_add_u32 s67, s67, 2039808\n\t"
        "s_sub_u32 s71, s67, %[ts]\n\t"
        "s_cselect_b32 s67, s67, s71\n\t"
        "s_add_u32 s68, s68, %[ts]\n\t"
        "s_add_u32 s64, s68, s67\n\t"
        "s_add_u32 m0, %[sl0], 76800\n\t"
        "s_nop 0\n\t"
        "buffer_load_dwordx4 %[wo3], %[rw], s65 offen lds\n\t"
        "buffer_load_dwordx4 %[wo4], %[rw], s65 offen offset:1024 lds\n\t"
        "buffer_load_dwordx4 %[wo5], %[rw], s65 offen offset:2048 lds\n\t"
        "s_add_u32 s65, s65, 24576\n\t"
        "s_add_u32 m0, %[sl0], 24576\n\t"
        "s_nop 0\n\t"
        "buffer_load_dwordx4 %[xo0], %[rx], s64 offen nt lds\n\t"
        "buffer_load_dwordx4 %[xo1], %[rx], s64 offen offset:1024 nt lds\n\t"
        "buffer_load_dwordx4 %[xo2], %[rx], s64 offen offset:2048 nt lds\n\t"
        "s_add_u32 m0, %[sl0], 98304\n\t"
        "s_nop 0\n\t"
        "buffer_load_dwordx4 %[wo0], %[rw], s65 offen lds\n\t"
        "buffer_load_dwordx4 %[wo1], %[rw], s65 offen offset:1024 lds\n\t"
        "buffer_load_dwordx4 %[wo2], %[rw], s65 offen offset:2048 lds\n\t"
        "s_add_u32 m0, %[sl0], 27648\n\t"
        "s_nop 0\n\t"
        "buffer_load_dwordx4 %[xo3], %[rx], s64 offen nt lds\n\t"
        "buffer_load_dwordx4 %[xo4], %[rx], s64 offen offset:1024 nt lds\n\t"
        "buffer_load_dwordx4 %[xo5], %[rx], s64 offen offset:2048 nt lds\n\t"
        "s_add_u32 s67, s67, 2039808\n\t"
        "s_sub_u32 s71, s67, %[ts]\n\t"
        "s_cselect_b32 s67, s67, s71\n\t"
        "s_add_u32 s68, s68, %[ts]\n\t"
        "s_add_u32 s64, s68, s67\n\t"
        "s_add_u32 m0, %[sl0], 49152\n\t"
        "s_nop 0\n\t"
        "buffer_load_dwordx4 %[xo0], %[rx], s64 offen nt lds\n\t"
        "buffer_load_dwordx4 %[xo1], %[rx], s64 offen offset:1024 nt lds\n\t"
        "buffer_load_dwordx4 %[xo2], %[rx], s64 offen offset:2048 nt lds\n\t"
        "s_add_u32 m0, %[sl0], 101376\n\t"
        "s_nop 0\n\t"
        "buffer_load_dwordx4 %[wo3], %[rw], s65 offen lds\n\t"
        "buffer_load_dwordx4 %[wo4], %[rw], s65 offen offset:1024 lds\n\t"
        "buffer_load_dwordx4 %[wo5], %[rw], s65 offen offset:2048 lds\n\t"
        "s_add_u32 s65, s65, 24576\n\t"
        "s_add_u32 m0, %[sl0], 52224\n\t"
        "s_nop 0\n\t"
        "buffer_load_dwordx4 %[xo3], %[rx], s64 offen nt lds\n\t"
        "buffer_load_dwordx4 %[xo4], %[rx], s64 offen offset:1024 nt lds\n\t"
        "buffer_load_dwordx4 %[xo5], %[rx], s64 offen offset:2048 nt lds\n\t"
        "s_add_u32 s67, s67, 2039808\n\t"
        "s_sub_u32 s71, s67, %[ts]\n\t"
        "s_cselect_b32 s67, s67, s71\n\t"
        "s_add_u32 s68, s68, %[ts]\n\t"
        "s_add_u32 s64, s68, s67\n\t"
        "v_lshrrev_b32 v0, 4, %[lane]\n\t"
        "v_and_b32 v1, 15, %[lane]\n\t"
        "v_mul_u32_u24 v2, 384, v1\n\t"
        "v_add_u32 v2, %[sl0], v2\n\t"
        "v_lshrrev_b32 v3, 1, v1\n\t"
        "v_xor_b32 v4, v0, v3\n\t"
        "v_lshl_add_u32 v212, v4, 4, v2\n\t"
        "v_xor_b32 v213, 64, v212\n\t"
        "v_lshrrev_b32 v4, 1, v0\n\t"
        "v_xor_b32 v4, v4, v3\n\t"
        "v_lshlrev_b32 v5, 4, v4\n\t"
        "v_and_b32 v4, 1, v0\n\t"
        "v_lshl_add_u32 v5, v4, 3, v5\n\t"
        "v_add_u32 v208, v5, v2\n\t"
        "v_lshl_add_u32 v215, v1, 7, v5\n\t"
        "v_add_u32 v215, %[stg], v215\n\t"
        "v_lshrrev_b32 v2, 2, v1\n\t"
        "v_lshl_add_u32 v3, v0, 3, v2\n\t"
        "v_mul_u32_u24 v3, 384, v3\n\t"
        "v_and_b32 v2, 2, v2\n\t"
        "v_lshl_or_b32 v2, v4, 2, v2\n\t"
        "v_lshl_add_u32 v3, v2, 4, v3\n\t"
        "v_and_b32 v2, 3, %[lane]\n\t"
        "v_lshl_add_u32 v3, v2, 3, v3\n\t"
        "v_add_u32 v204, %[wbase], v3\n\t"
        "v_xor_b32 v205, 32, v204\n\t"
        "v_xor_b32 v209, 32, v208\n\t"
        "v_xor_b32 v216, 32, v215\n\t"
        "v_xor_b32 v206, 64, v204\n\t"
        "v_xor_b32 v210, 64, v208\n\t"
        "v_xor_b32 v217, 64, v215\n\t"
        "v_xor_b32 v207, 96, v204\n\t"
        "v_xor_b32 v211, 96, v208\n\t"
        "v_xor_b32 v218, 96, v215\n\t"
        "v_lshlrev_b32 v2, 4, %[lane]\n\t"
        "v_add_u32 v214, %[sl0], v2\n\t"
        "v_add_u32 v219, %[stg], v2\n\t"
        "v_lshrrev_b32 v2, 3, %[lane]\n\t"
        "v_mul_u32_u24 v3, 384, v2\n\t"
        "v_and_b32 v4, 7, %[lane]\n\t"
        "v_xor_b32 v4, v4, v0\n\t"
        "v_lshl_add_u32 v220, v4, 4, v3\n\t"
        "v_xor_b32 v221, 64, v220\n\t"
        "v_add_u32 v221, 3072, v221\n\t"
        "v_lshlrev_b32 v202, 3, v0\n\t"
        "v_add_u32 v202, %[wbase], v202\n\t"
        "v_add_u32 v202, 73728, v202\n\t"
        "s_mov_b32 s60, %[olo]\n\t"
        "s_mov_b32 s61, %[ohi]\n\t"
        "s_mov_b32 s62, %[nb]\n\t"
        "s_mov_b32 s63, 0x20000\n\t"
        "s_mov_b32 s66, %[t0]\n\t"
        "s_mov_b32 s69, %[t0]\n\t"
        "s_mov_b32 s70, 0\n\t"
        "s_waitcnt vmcnt(18)\n\t"
        "s_barrier\n\t"
        "ds_read_b64 v[46:47], v202 offset:0\n\t"
        "ds_read_b64 v[50:51], v202 offset:32\n\t"
        "ds_read_b64 v[54:55], v202 offset:64\n\t"
        "ds_read_b64 v[58:59], v202 offset:96\n\t"
        "ds_read_b64 v[62:63], v202 offset:128\n\t"
        "ds_read_b64 v[66:67], v202 offset:160\n\t"
        "ds_read_b64 v[70:71], v202 offset:192\n\t"
        "ds_read_b64 v[74:75], v202 offset:224\n\t"
        "ds_read_b64 v[78:79], v202 offset:256\n\t"
        "ds_read_b64 v[82:83], v202 offset:288\n\t"
        "ds_read_b64 v[86:87], v202 offset:320\n\t"
        "ds_read_b64 v[90:91], v202 offset:352\n\t"
        "ds_read_b128 v[0:3], v212 offset:0\n\t"
        "ds_read_b128 v[4:7], v213 offset:0\n\t"
        "ds_read_b128 v[8:11], v212 offset:128\n\t"
        "ds_read_b128 v[12:15], v213 offset:128\n\t"
        "ds_read_b128 v[16:19], v212 offset:256\n\t"
        "ds_read_b128 v[20:23], v213 offset:256\n\t"
        "ds_read_b64_tr_b16 a[0:1], v204 offset:0\n\t"
        "ds_read_b64_tr_b16 a[2:3], v204 offset:1536\n\t"
        "ds_read_b64_tr_b16 a[4:5], v205 offset:0\n\t"
        "ds_read_b64_tr_b16 a[6:7], v205 offset:1536\n\t"
        "ds_read_b64_tr_b16 a[8:9], v206 offset:0\n\t"
        "ds_read_b64_tr_b16 a[10:11], v206 offset:1536\n\t"
        "ds_read_b64_tr_b16 a[12:13], v207 offset:0\n\t"
        "ds_read_b64_tr_b16 a[14:15], v207 offset:1536\n\t"
        "ds_read_b64_tr_b16 a[16:17], v204 offset:128\n\t"
        "ds_read_b64_tr_b16 a[18:19], v204 offset:1664\n\t"
        "ds_read_b64_tr_b16 a[20:21], v205 offset:128\n\t"
        "ds_read_b64_tr_b16 a[22:23], v205 offset:1664\n\t"
        "ds_read_b64_tr_b16 a[24:25], v206 offset:128\n\t"
        "ds_read_b64_tr_b16 a[26:27], v206 offset:1664\n\t"
        "ds_read_b64_tr_b16 a[28:29], v207 offset:128\n\t"
        "ds_read_b64_tr_b16 a[30:31], v207 offset:1664\n\t"
        "ds_read_b64_tr_b16 a[32:33], v204 offset:256\n\t"
        "ds_read_b64_tr_b16 a[34:35], v204 offset:1792\n\t"
        "ds_read_b64_tr_b16 a[36:37], v205 offset:256\n\t"
        "ds_read_b64_tr_b16 a[38:39], v205 offset:1792\n\t"
        "ds_read_b64_tr_b16 a[40:41], v206 offset:256\n\t"
        "ds_read_b64_tr_b16 a[42:43], v206 offset:1792\n\t"
        "ds_read_b64_tr_b16 a[44:45], v207 offset:256\n\t"
        "ds_read_b64_tr_b16 a[46:47], v207 offset:1792\n\t"
        "ds_read_b64_tr_b16 a[48:49], v204 offset:12288\n\t"
        "ds_read_b64_tr_b16 a[50:51], v204 offset:13824\n\t"
        "ds_read_b64_tr_b16 a[52:53], v205 offset:12288\n\t"
        "ds_read_b64_tr_b16 a[54:55], v205 offset:13824\n\t"
        "ds_read_b64_tr_b16 a[56:57], v206 offset:12288\n\t"
        "ds_read_b64_tr_b16 a[58:59], v206 offset:13824\n\t"
        "ds_read_b64_tr_b16 a[60:61], v207 offset:12288\n\t"
        "ds_read_b64_tr_b16 a[62:63], v207 offset:13824\n\t"
        "ds_read_b64_tr_b16 a[64:65], v204 offset:12416\n\t"
        "ds_read_b64_tr_b16 a[66:67], v204 offset:13952\n\t"
        "ds_read_b64_tr_b16 a[68:69], v205 offset:12416\n\t"
        "ds_read_b64_tr_b16 a[70:71], v205 offset:13952\n\t"
        "ds_read_b64_tr_b16 a[72:73], v206 offset:12416\n\t"
        "ds_read_b64_tr_b16 a[74:75], v206 offset:13952\n\t"
        "ds_read_b64_tr_b16 a[76:77], v207 offset:12416\n\t"
        "ds_read_b64_tr_b16 a[78:79], v207 offset:13952\n\t"
        "ds_read_b64_tr_b16 a[80:81], v204 offset:12544\n\t"
        "ds_read_b64_tr_b16 a[82:83], v204 offset:14080\n\t"
        "ds_read_b64_tr_b16 a[84:85], v205 offset:12544\n\t"
        "ds_read_b64_tr_b16 a[86:87], v205 offset:14080\n\t"
        "ds_read_b64_tr_b16 a[88:89], v206 offset:12544\n\t"
        "ds_read_b64_tr_b16 a[90:91], v206 offset:14080\n\t"
        "ds_read_b64_tr_b16 a[92:93], v207 offset:12544\n\t"
        "ds_read_b64_tr_b16 a[94:95], v207 offset:14080\n\t"
        "s_add_u32 m0, %[sl0], 122880\n\t"
        "s_nop 0\n\t"
        "buffer_load_dwordx4 %[wo0], %[rw], s65 offen lds\n\t"
        "buffer_load_dwordx4 %[wo1], %[rw], s65 offen offset:1024 lds\n\t"
        "buffer_load_dwordx4 %[wo2], %[rw], s65 offen offset:2048 lds\n\t"
        "s_add_u32 m0, %[sl0], 125952\n\t"
        "s_nop 0\n\t"
        "buffer_load_dwordx4 %[wo3], %[rw], s65 offen lds\n\t"
        "s_waitcnt lgkmcnt(0)\n\t"
        "v_lshlrev_b32 v44, 16, v46\n\t"
        "v_and_b32 v45, 0xffff0000, v46\n\t"
        "v_lshlrev_b32 v46, 16, v47\n\t"
        "v_and_b32 v47, 0xffff0000, v47\n\t"
        "v_lshlrev_b32 v48, 16, v50\n\t"
        "v_and_b32 v49, 0xffff0000, v50\n\t"
        "v_lshlrev_b32 v50, 16, v51\n\t"
        "v_and_b32 v51, 0xffff0000, v51\n\t"
        "v_lshlrev_b32 v52, 16, v54\n\t"
        "v_and_b32 v53, 0xffff0000, v54\n\t"
        "v_lshlrev_b32 v54, 16, v55\n\t"
        "v_and_b32 v55, 0xffff0000, v55\n\t"
        "v_lshlrev_b32 v56, 16, v58\n\t"
        "v_and_b32 v57, 0xffff0000, v58\n\t"
        "v_lshlrev_b32 v58, 16, v59\n\t"
        "v_and_b32 v59, 0xffff0000, v59\n\t"
        "v_lshlrev_b32 v60, 16, v62\n\t"
        "v_and_b32 v61, 0xffff0000, v62\n\t"
        "v_lshlrev_b32 v62, 16, v63\n\t"
        "v_and_b32 v63, 0xffff0000, v63\n\t"
        "v_lshlrev_b32 v64, 16, v66\n\t"
        "v_and_b32 v65, 0xffff0000, v66\n\t"
        "v_lshlrev_b32 v66, 16, v67\n\t"
        "v_and_b32 v67, 0xffff0000, v67\n\t"
        "v_lshlrev_b32 v68, 16, v70\n\t"
        "v_and_b32 v69, 0xffff0000, v70\n\t"
        "v_lshlrev_b32 v70, 16, v71\n\t"
        "v_and_b32 v71, 0xffff0000, v71\n\t"
        "v_lshlrev_b32 v72, 16, v74\n\t"
        "v_and_b32 v73, 0xffff0000, v74\n\t"
        "v_lshlrev_b32 v74, 16, v75\n\t"
        "v_and_b32 v75, 0xffff0000, v75\n\t"
        "v_lshlrev_b32 v76, 16, v78\n\t"
        "v_and_b32 v77, 0xffff0000, v78\n\t"
        "v_lshlrev_b32 v78, 16, v79\n\t"
        "v_and_b32 v79, 0xffff0000, v79\n\t"
        "v_lshlrev_b32 v80, 16, v82\n\t"
        "v_and_b32 v81, 0xffff0000, v82\n\t"
        "v_lshlrev_b32 v82, 16, v83\n\t"
        "v_and_b32 v83, 0xffff0000, v83\n\t"
        "v_lshlrev_b32 v84, 16, v86\n\t"
        "v_and_b32 v85, 0xffff0000, v86\n\t"
        "v_lshlrev_b32 v86, 16, v87\n\t"
        "v_and_b32 v87, 0xffff0000, v87\n\t"
        "v_lshlrev_b32 v88, 16, v90\n\t"
        "v_and_b32 v89, 0xffff0000, v90\n\t"
        "v_lshlrev_b32 v90, 16, v91\n\t"
        "v_and_b32 v91, 0xffff0000, v91\n\t"
        "s_nop 1\n\t"
        "s_add_u32 m0, %[sl0], 0\n\t"
        "s_nop 0\n\t"
        "buffer_load_dwordx4 %[xo0], %[rx], s64 offen nt lds\n\t"
        "v_mfma_f32_16x16x32_bf16 v[148:151], a[0:3], v[0:3], v[44:47]\n\t"
        "s_nop 5\n\t"
        "v_mfma_f32_16x16x32_bf16 v[152:155], a[4:7], v[0:3], v[48:51]\n\t"
        "s_nop 5\n\t"
        "v_mfma_f32_16x16x32_bf16 v[156:159], a[8:11], v[0:3], v[52:55]\n\t"
        "s_nop 5\n\t"
        "buffer_load_dwordx4 %[xo1], %[rx], s64 offen offset:1024 nt lds\n\t"
        "v_mfma_f32_16x16x32_bf16 v[160:163], a[12:15], v[0:3], v[56:59]\n\t"
        "s_nop 5\n\t"
        "v_mfma_f32_16x16x32_bf16 v[164:167], a[16:19], v[0:3], v[60:63]\n\t"
        "s_nop 5\n\t"
        "v_mfma_f32_16x16x32_bf16 v[168:171], a[20:23], v[0:3], v[64:67]\n\t"
        "s_nop 5\n\t"
        "buffer_load_dwordx4 %[xo2], %[rx], s64 offen offset:2048 nt lds\n\t"
        "v_mfma_f32_16x16x32_bf16 v[172:175], a[24:27], v[0:3], v[68:71]\n\t"
        "s_nop 5\n\t"
        "v_mfma_f32_16x16x32_bf16 v[176:179], a[28:31], v[0:3], v[72:75]\n\t"
        "s_nop 5\n\t"
        "v_mfma_f32_16x16x32_bf16 v[180:183], a[32:35], v[0:3], v[76:79]\n\t"
        "s_nop 5\n\t"
        "s_add_u32 m0, %[sl0], 125952\n\t"
        "s_nop 0\n\t"
        "buffer_load_dwordx4 %[wo4], %[rw], s65 offen offset:1024 lds\n\t"
        "v_mfma_f32_16x16x32_bf16 v[184:187], a[36:39], v[0:3], v[80:83]\n\t"
        "s_nop 5\n\t"
        "v_mfma_f32_16x16x32_bf16 v[188:191], a[40:43], v[0:3], v[84:87]\n\t"
        "s_nop 5\n\t"
        "v_mfma_f32_16x16x32_bf16 v[192:195], a[44:47], v[0:3], v[88:91]\n\t"
        "s_nop 5\n\t"
        "buffer_load_dwordx4 %[wo5], %[rw], s65 offen offset:2048 lds\n\t"
        "v_mfma_f32_16x16x32_bf16 v[148:151], a[48:51], v[4:7], v[148:151]\n\t"
        "s_nop 5\n\t"
        "v_mfma_f32_16x16x32_bf16 v[152:155], a[52:55], v[4:7], v[152:155]\n\t"
        "s_nop 5\n\t"
        "v_mfma_f32_16x16x32_bf16 v[156:159], a[56:59], v[4:7], v[156:159]\n\t"
        "s_nop 5\n\t"
        "s_add_u32 m0, %[sl0], 3072\n\t"
        "s_nop 0\n\t"
        "buffer_load_dwordx4 %[xo3], %[rx], s64 offen nt lds\n\t"
        "v_mfma_f32_16x16x32_bf16 v[160:163], a[60:63], v[4:7], v[160:163]\n\t"
        "s_nop 5\n\t"
        "v_mfma_f32_16x16x32_bf16 v[164:167], a[64:67], v[4:7], v[164:167]\n\t"
        "s_nop 5\n\t"
        "v_mfma_f32_16x16x32_bf16 v[168:171], a[68:71], v[4:7], v[168:171]\n\t"
        "s_nop 5\n\t"
        "buffer_load_dwordx4 %[xo4], %[rx], s64 offen offset:1024 nt lds\n\t"
        "v_mfma_f32_16x16x32_bf16 v[172:175], a[72:75], v[4:7], v[172:175]\n\t"
        "s_nop 5\n\t"
        "v_mfma_f32_16x16x32_bf16 v[176:179], a[76:79], v[4:7], v[176:179]\n\t"
        "s_nop 5\n\t"
        "v_mfma_f32_16x16x32_bf16 v[180:183], a[80:83], v[4:7], v[180:183]\n\t"
        "s_nop 5\n\t"
        "buffer_load_dwordx4 %[xo5], %[rx], s64 offen offset:2048 nt lds\n\t"
        "s_add_u32 s67, s67, 2039808\n\t"
        "s_sub_u32 s71, s67, %[ts]\n\t"
        "s_cselect_b32 s67, s67, s71\n\t"
        "s_add_u32 s68, s68, %[ts]\n\t"
        "s_add_u32 s64, s68, s67\n\t"
        "v_mfma_f32_16x16x32_bf16 v[184:187], a[84:87], v[4:7], v[184:187]\n\t"
        "s_nop 5\n\t"
        "v_mfma_f32_16x16x32_bf16 v[188:191], a[88:91], v[4:7], v[188:191]\n\t"
        "s_nop 5\n\t"
        "v_mfma_f32_16x16x32_bf16 v[192:195], a[92:95], v[4:7], v[192:195]\n\t"
        "s_nop 5\n\t"
        "s_waitcnt vmcnt(15)\n\t"
        "s_barrier\n\t"
        "ds_read_b64_tr_b16 a[96:97], v204 offset:24576\n\t"
        "ds_read_b64_tr_b16 a[98:99], v204 offset:26112\n\t"
        "ds_read_b64_tr_b16 a[100:101], v205 offset:24576\n\t"
        "ds_read_b64_tr_b16 a[102:103], v205 offset:26112\n\t"
        "ds_read_b64_tr_b16 a[104:105], v206 offset:24576\n\t"
        "ds_read_b64_tr_b16 a[106:107], v206 offset:26112\n\t"
        "ds_read_b64_tr_b16 a[108:109], v207 offset:24576\n\t"
        "ds_read_b64_tr_b16 a[110:111], v207 offset:26112\n\t"
        "ds_read_b64_tr_b16 a[112:113], v204 offset:24704\n\t"
        "ds_read_b64_tr_b16 a[114:115], v204 offset:26240\n\t"
        "ds_read_b64_tr_b16 a[116:117], v205 offset:24704\n\t"
        "ds_read_b64_tr_b16 a[118:119], v205 offset:26240\n\t"
        "ds_read_b64_tr_b16 a[120:121], v206 offset:24704\n\t"
        "ds_read_b64_tr_b16 a[122:123], v206 offset:26240\n\t"
        "ds_read_b64_tr_b16 a[124:125], v207 offset:24704\n\t"
        "ds_read_b64_tr_b16 a[126:127], v207 offset:26240\n\t"
        "ds_read_b64_tr_b16 a[128:129], v204 offset:24832\n\t"
        "ds_read_b64_tr_b16 a[130:131], v204 offset:26368\n\t"
        "ds_read_b64_tr_b16 a[132:133], v205 offset:24832\n\t"
        "ds_read_b64_tr_b16 a[134:135], v205 offset:26368\n\t"
        "ds_read_b64_tr_b16 a[136:137], v206 offset:24832\n\t"
        "ds_read_b64_tr_b16 a[138:139], v206 offset:26368\n\t"
        "ds_read_b64_tr_b16 a[140:141], v207 offset:24832\n\t"
        "ds_read_b64_tr_b16 a[142:143], v207 offset:26368\n\t"
        "ds_read_b64_tr_b16 a[144:145], v204 offset:36864\n\t"
        "ds_read_b64_tr_b16 a[146:147], v204 offset:38400\n\t"
        "ds_read_b64_tr_b16 a[148:149], v205 offset:36864\n\t"
        "ds_read_b64_tr_b16 a[150:151], v205 offset:38400\n\t"
        "ds_read_b64_tr_b16 a[152:153], v206 offset:36864\n\t"
        "ds_read_b64_tr_b16 a[154:155], v206 offset:38400\n\t"
        "ds_read_b64_tr_b16 a[156:157], v207 offset:36864\n\t"
        "ds_read_b64_tr_b16 a[158:159], v207 offset:38400\n\t"
        "ds_read_b64_tr_b16 a[160:161], v204 offset:36992\n\t"
        "ds_read_b64_tr_b16 a[162:163], v204 offset:38528\n\t"
        "ds_read_b64_tr_b16 a[164:165], v205 offset:36992\n\t"
        "ds_read_b64_tr_b16 a[166:167], v205 offset:38528\n\t"
        "ds_read_b64_tr_b16 a[168:169], v206 offset:36992\n\t"
        "ds_read_b64_tr_b16 a[170:171], v206 offset:38528\n\t"
        "ds_read_b64_tr_b16 a[172:173], v207 offset:36992\n\t"
        "ds_read_b64_tr_b16 a[174:175], v207 offset:38528\n\t"
        "ds_read_b64_tr_b16 a[176:177], v204 offset:37120\n\t"
        "ds_read_b64_tr_b16 a[178:179], v204 offset:38656\n\t"
        "ds_read_b64_tr_b16 a[180:181], v205 offset:37120\n\t"
        "ds_read_b64_tr_b16 a[182:183], v205 offset:38656\n\t"
        "ds_read_b64_tr_b16 a[184:185], v206 offset:37120\n\t"
        "ds_read_b64_tr_b16 a[186:187], v206 offset:38656\n\t"
        "ds_read_b64_tr_b16 a[188:189], v207 offset:37120\n\t"
        "ds_read_b64_tr_b16 a[190:191], v207 offset:38656\n\t"
        "s_add_u32 m0, %[sl0], 73728\n\t"
        "s_nop 0\n\t"
        "buffer_load_dwordx4 %[xo0], %[rx], s64 offen nt lds\n\t"
        "buffer_load_dwordx4 %[xo1], %[rx], s64 offen offset:1024 nt lds\n\t"
        "buffer_load_dwordx4 %[xo2], %[rx], s64 offen offset:2048 nt lds\n\t"
        "s_waitcnt lgkmcnt(0)\n\t"
        "s_add_u32 m0, %[sl0], 76800\n\t"
        "s_nop 0\n\t"
        "buffer_load_dwordx4 %[xo3], %[rx], s64 offen nt lds\n\t"
        "v_mfma_f32_16x16x32_bf16 v[148:151], a[96:99], v[8:11], v[148:151]\n\t"
        "s_nop 5\n\t"
        "v_mfma_f32_16x16x32_bf16 v[152:155], a[100:103], v[8:11], v[152:155]\n\t"
        "s_nop 5\n\t"
        "v_mfma_f32_16x16x32_bf16 v[156:159], a[104:107], v[8:11], v[156:159]\n\t"
        "s_nop 5\n\t"
        "v_mfma_f32_16x16x32_bf16 v[160:163], a[108:111], v[8:11], v[160:163]\n\t"
        "s_nop 5\n\t"
        "v_mfma_f32_16x16x32_bf16 v[164:167], a[112:115], v[8:11], v[164:167]\n\t"
        "s_nop 5\n\t"
        "v_mfma_f32_16x16x32_bf16 v[168:171], a[116:119], v[8:11], v[168:171]\n\t"
        "s_nop 5\n\t"
        "v_mfma_f32_16x16x32_bf16 v[172:175], a[120:123], v[8:11], v[172:175]\n\t"
        "s_nop 5\n\t"
        "v_mfma_f32_16x16x32_bf16 v[176:179], a[124:127], v[8:11], v[176:179]\n\t"
        "s_nop 5\n\t"
        "buffer_load_dwordx4 %[xo4], %[rx], s64 offen offset:1024 nt lds\n\t"
        "v_mfma_f32_16x16x32_bf16 v[180:183], a[128:131], v[8:11], v[180:183]\n\t"
        "s_nop 5\n\t"
        "v_mfma_f32_16x16x32_bf16 v[184:187], a[132:135], v[8:11], v[184:187]\n\t"
        "s_nop 5\n\t"
        "v_mfma_f32_16x16x32_bf16 v[188:191], a[136:139], v[8:11], v[188:191]\n\t"
        "s_nop 5\n\t"
        "v_mfma_f32_16x16x32_bf16 v[192:195], a[140:143], v[8:11], v[192:195]\n\t"
        "s_nop 5\n\t"
        "v_mfma_f32_16x16x32_bf16 v[148:151], a[144:147], v[12:15], v[148:151]\n\t"
        "s_nop 5\n\t"
        "v_mfma_f32_16x16x32_bf16 v[152:155], a[148:151], v[12:15], v[152:155]\n\t"
        "s_nop 5\n\t"
        "v_mfma_f32_16x16x32_bf16 v[156:159], a[152:155], v[12:15], v[156:159]\n\t"
        "s_nop 5\n\t"
        "v_mfma_f32_16x16x32_bf16 v[160:163], a[156:159], v[12:15], v[160:163]\n\t"
        "s_nop 5\n\t"
        "buffer_load_dwordx4 %[xo5], %[rx], s64 offen offset:2048 nt lds\n\t"
        "s_add_u32 s67, s67, 2039808\n\t"
        "s_sub_u32 s71, s67, %[ts]\n\t"
        "s_cselect_b32 s67, s67, s71\n\t"
        "s_add_u32 s68, s68, %[ts]\n\t"
        "s_add_u32 s64, s68, s67\n\t"
        "v_mfma_f32_16x16x32_bf16 v[164:167], a[160:163], v[12:15], v[164:167]\n\t"
        "s_nop 5\n\t"
        "v_mfma_f32_16x16x32_bf16 v[168:171], a[164:167], v[12:15], v[168:171]\n\t"
        "s_nop 5\n\t"
        "v_mfma_f32_16x16x32_bf16 v[172:175], a[168:171], v[12:15], v[172:175]\n\t"
        "s_nop 5\n\t"
        "v_mfma_f32_16x16x32_bf16 v[176:179], a[172:175], v[12:15], v[176:179]\n\t"
        "s_nop 5\n\t"
        "v_mfma_f32_16x16x32_bf16 v[180:183], a[176:179], v[12:15], v[180:183]\n\t"
        "s_nop 5\n\t"
        "v_mfma_f32_16x16x32_bf16 v[184:187], a[180:183], v[12:15], v[184:187]\n\t"
        "s_nop 5\n\t"
        "v_mfma_f32_16x16x32_bf16 v[188:191], a[184:187], v[12:15], v[188:191]\n\t"
        "s_nop 5\n\t"
        "v_mfma_f32_16x16x32_bf16 v[192:195], a[188:191], v[12:15], v[192:195]\n\t"
        "s_nop 5\n\t"
        "s_waitcnt vmcnt(9)\n\t"
        "s_barrier\n\t"
        "ds_read_b64_tr_b16 a[192:193], v204 offset:49152\n\t"
        "ds_read_b64_tr_b16 a[194:195], v204 offset:50688\n\t"
        "ds_read_b64_tr_b16 a[196:197], v205 offset:49152\n\t"
        "ds_read_b64_tr_b16 a[198:199], v205 offset:50688\n\t"
        "ds_read_b64_tr_b16 a[200:201], v206 offset:49152\n\t"
        "ds_read_b64_tr_b16 a[202:203], v206 offset:50688\n\t"
        "ds_read_b64_tr_b16 a[204:205], v207 offset:49152\n\t"
        "ds_read_b64_tr_b16 a[206:207], v207 offset:50688\n\t"
        "ds_read_b64_tr_b16 a[208:209], v204 offset:49280\n\t"
        "ds_read_b64_tr_b16 a[210:211], v204 offset:50816\n\t"
        "ds_read_b64_tr_b16 a[212:213], v205 offset:49280\n\t"
        "ds_read_b64_tr_b16 a[214:215], v205 offset:50816\n\t"
        "ds_read_b64_tr_b16 a[216:217], v206 offset:49280\n\t"
        "ds_read_b64_tr_b16 a[218:219], v206 offset:50816\n\t"
        "ds_read_b64_tr_b16 a[220:221], v207 offset:49280\n\t"
        "ds_read_b64_tr_b16 a[222:223], v207 offset:50816\n\t"
        "ds_read_b64_tr_b16 a[224:225], v204 offset:49408\n\t"
        "ds_read_b64_tr_b16 a[226:227], v204 offset:50944\n\t"
        "ds_read_b64_tr_b16 a[228:229], v205 offset:49408\n\t"
        "ds_read_b64_tr_b16 a[230:231], v205 offset:50944\n\t"
        "ds_read_b64_tr_b16 a[232:233], v206 offset:49408\n\t"
        "ds_read_b64_tr_b16 a[234:235], v206 offset:50944\n\t"
        "ds_read_b64_tr_b16 a[236:237], v207 offset:49408\n\t"
        "ds_read_b64_tr_b16 a[238:239], v207 offset:50944\n\t"
        "ds_read_b64_tr_b16 a[240:241], v204 offset:61440\n\t"
        "ds_read_b64_tr_b16 a[242:243], v204 offset:62976\n\t"
        "ds_read_b64_tr_b16 a[244:245], v205 offset:61440\n\t"
        "ds_read_b64_tr_b16 a[246:247], v205 offset:62976\n\t"
        "ds_read_b64_tr_b16 a[248:249], v206 offset:61440\n\t"
        "ds_read_b64_tr_b16 a[250:251], v206 offset:62976\n\t"
        "ds_read_b64_tr_b16 a[252:253], v207 offset:61440\n\t"
        "ds_read_b64_tr_b16 a[254:255], v207 offset:62976\n\t"
        "ds_read_b64_tr_b16 v[92:93], v204 offset:61568\n\t"
        "ds_read_b64_tr_b16 v[94:95], v204 offset:63104\n\t"
        "ds_read_b64_tr_b16 v[96:97], v205 offset:61568\n\t"
        "ds_read_b64_tr_b16 v[98:99], v205 offset:63104\n\t"
        "ds_read_b64_tr_b16 v[100:101], v206 offset:61568\n\t"
        "ds_read_b64_tr_b16 v[102:103], v206 offset:63104\n\t"
        "ds_read_b64_tr_b16 v[104:105], v207 offset:61568\n\t"
        "ds_read_b64_tr_b16 v[106:107], v207 offset:63104\n\t"
        "ds_read_b64_tr_b16 v[108:109], v204 offset:61696\n\t"
        "ds_read_b64_tr_b16 v[110:111], v204 offset:63232\n\t"
        "ds_read_b64_tr_b16 v[112:113], v205 offset:61696\n\t"
        "ds_read_b64_tr_b16 v[114:115], v205 offset:63232\n\t"
        "ds_read_b64_tr_b16 v[116:117], v206 offset:61696\n\t"
        "ds_read_b64_tr_b16 v[118:119], v206 offset:63232\n\t"
        "ds_read_b64_tr_b16 v[120:121], v207 offset:61696\n\t"
        "ds_read_b64_tr_b16 v[122:123], v207 offset:63232\n\t"
        "v_add_u32 v196, 24576, v212\n\t"
        "v_add_u32 v197, 24576, v213\n\t"
        "ds_read_b128 v[0:3], v196 offset:0\n\t"
        "ds_read_b128 v[4:7], v197 offset:0\n\t"
        "ds_read_b128 v[8:11], v196 offset:128\n\t"
        "ds_read_b128 v[12:15], v197 offset:128\n\t"
        "s_add_u32 m0, %[sl0], 98304\n\t"
        "s_nop 0\n\t"
        "buffer_load_dwordx4 %[xo0], %[rx], s64 offen nt lds\n\t"
        "buffer_load_dwordx4 %[xo1], %[rx], s64 offen offset:1024 nt lds\n\t"
        "buffer_load_dwordx4 %[xo2], %[rx], s64 offen offset:2048 nt lds\n\t"
        "s_add_u32 m0, %[sl0], 101376\n\t"
        "s_nop 0\n\t"
        "buffer_load_dwordx4 %[xo3], %[rx], s64 offen nt lds\n\t"
        "buffer_load_dwordx4 %[xo4], %[rx], s64 offen offset:1024 nt lds\n\t"
        "buffer_load_dwordx4 %[xo5], %[rx], s64 offen offset:2048 nt lds\n\t"
        "s_add_u32 s67, s67, 2039808\n\t"
        "s_sub_u32 s71, s67, %[ts]\n\t"
        "s_cselect_b32 s67, s67, s71\n\t"
        "s_add_u32 s68, s68, %[ts]\n\t"
        "s_add_u32 s64, s68, s67\n\t"
        "s_waitcnt lgkmcnt(0)\n\t"
        "s_barrier\n\t"
        "v_mfma_f32_16x16x32_bf16 v[148:151], a[192:195], v[16:19], v[148:151]\n\t"
        "s_nop 3\n\t"
        "v_mfma_f32_16x16x32_bf16 v[148:151], a[240:243], v[20:23], v[148:151]\n\t"
        "s_add_u32 m0, %[sl0], 122880\n\t"
        "s_nop 0\n\t"
        "buffer_load_dwordx4 %[xo0], %[rx], s64 offen nt lds\n\t"
        "s_nop 3\n\t"
        "v_mfma_f32_16x16x32_bf16 v[152:155], a[196:199], v[16:19], v[152:155]\n\t"
        "s_nop 3\n\t"
        "v_mfma_f32_16x16x32_bf16 v[152:155], a[244:247], v[20:23], v[152:155]\n\t"
        "s_nop 3\n\t"
        "v_mfma_f32_16x16x32_bf16 v[156:159], a[200:203], v[16:19], v[156:159]\n\t"
        "s_nop 3\n\t"
        "v_mfma_f32_16x16x32_bf16 v[156:159], a[248:251], v[20:23], v[156:159]\n\t"
        "s_nop 3\n\t"
        "v_mfma_f32_16x16x32_bf16 v[160:163], a[204:207], v[16:19], v[160:163]\n\t"
        "s_nop 3\n\t"
        "v_mfma_f32_16x16x32_bf16 v[160:163], a[252:255], v[20:23], v[160:163]\n\t"
        "buffer_load_dwordx4 %[xo1], %[rx], s64 offen offset:1024 nt lds\n\t"
        "v_cvt_pk_bf16_f32 v40, v148, v149\n\t"
        "v_cvt_pk_bf16_f32 v41, v150, v151\n\t"
        "ds_write_b64 v215, v[40:41]\n\t"
        "v_mfma_f32_16x16x32_bf16 v[164:167], a[208:211], v[16:19], v[164:167]\n\t"
        "s_nop 3\n\t"
        "v_mfma_f32_16x16x32_bf16 v[164:167], v[92:95], v[20:23], v[164:167]\n\t"
        "v_cvt_pk_bf16_f32 v42, v152, v153\n\t"
        "v_cvt_pk_bf16_f32 v43, v154, v155\n\t"
        "ds_write_b64 v216, v[42:43]\n\t"
        "v_mfma_f32_16x16x32_bf16 v[168:171], a[212:215], v[16:19], v[168:171]\n\t"
        "s_nop 3\n\t"
        "v_mfma_f32_16x16x32_bf16 v[168:171], v[96:99], v[20:23], v[168:171]\n\t"
        "v_cvt_pk_bf16_f32 v40, v156, v157\n\t"
        "v_cvt_pk_bf16_f32 v41, v158, v159\n\t"
        "ds_write_b64 v217, v[40:41]\n\t"
        "v_mfma_f32_16x16x32_bf16 v[172:175], a[216:219], v[16:19], v[172:175]\n\t"
        "s_nop 3\n\t"
        "v_mfma_f32_16x16x32_bf16 v[172:175], v[100:103], v[20:23], v[172:175]\n\t"
        "buffer_load_dwordx4 %[xo2], %[rx], s64 offen offset:2048 nt lds\n\t"
        "v_cvt_pk_bf16_f32 v42, v160, v161\n\t"
        "v_cvt_pk_bf16_f32 v43, v162, v163\n\t"
        "ds_write_b64 v218, v[42:43]\n\t"
        "ds_read_b128 v[124:127], v219 offset:0\n\t"
        "ds_read_b128 v[128:131], v219 offset:1024\n\t"
        "v_mfma_f32_16x16x32_bf16 v[176:179], a[220:223], v[16:19], v[176:179]\n\t"
        "s_nop 3\n\t"
        "v_mfma_f32_16x16x32_bf16 v[176:179], v[104:107], v[20:23], v[176:179]\n\t"
        "v_cvt_pk_bf16_f32 v40, v164, v165\n\t"
        "v_cvt_pk_bf16_f32 v41, v166, v167\n\t"
        "ds_write_b64 v215, v[40:41]\n\t"
        "v_mfma_f32_16x16x32_bf16 v[180:183], a[224:227], v[16:19], v[180:183]\n\t"
        "s_nop 3\n\t"
        "v_mfma_f32_16x16x32_bf16 v[180:183], v[108:111], v[20:23], v[180:183]\n\t"
        "v_cvt_pk_bf16_f32 v42, v168, v169\n\t"
        "v_cvt_pk_bf16_f32 v43, v170, v171\n\t"
        "ds_write_b64 v216, v[42:43]\n\t"
        "v_mfma_f32_16x16x32_bf16 v[184:187], a[228:231], v[16:19], v[184:187]\n\t"
        "s_nop 3\n\t"
        "v_mfma_f32_16x16x32_bf16 v[184:187], v[112:115], v[20:23], v[184:187]\n\t"
        "s_add_u32 m0, %[sl0], 125952\n\t"
        "s_nop 0\n\t"
        "buffer_load_dwordx4 %[xo3], %[rx], s64 offen nt lds\n\t"
        "v_cvt_pk_bf16_f32 v40, v172, v173\n\t"
        "v_cvt_pk_bf16_f32 v41, v174, v175\n\t"
        "ds_write_b64 v217, v[40:41]\n\t"
        "v_mfma_f32_16x16x32_bf16 v[188:191], a[232:235], v[16:19], v[188:191]\n\t"
        "s_nop 3\n\t"
        "v_mfma_f32_16x16x32_bf16 v[188:191], v[116:119], v[20:23], v[188:191]\n\t"
        "v_cvt_pk_bf16_f32 v42, v176, v177\n\t"
        "v_cvt_pk_bf16_f32 v43, v178, v179\n\t"
        "ds_write_b64 v218, v[42:43]\n\t"
        "ds_read_b128 v[132:135], v219 offset:0\n\t"
        "ds_read_b128 v[136:139], v219 offset:1024\n\t"
        "v_mfma_f32_16x16x32_bf16 v[192:195], a[236:239], v[16:19], v[192:195]\n\t"
        "s_nop 3\n\t"
        "v_mfma_f32_16x16x32_bf16 v[192:195], v[120:123], v[20:23], v[192:195]\n\t"
        "v_cvt_pk_bf16_f32 v40, v180, v181\n\t"
        "v_cvt_pk_bf16_f32 v41, v182, v183\n\t"
        "ds_write_b64 v215, v[40:41]\n\t"
        "ds_read_b128 v[16:19], v196 offset:256\n\t"
        "ds_read_b128 v[20:23], v197 offset:256\n\t"
        "s_memrealtime s[74:75]\n\t"
        "v_cvt_pk_bf16_f32 v42, v184, v185\n\t"
        "v_cvt_pk_bf16_f32 v43, v186, v187\n\t"
        "ds_write_b64 v216, v[42:43]\n\t"
        "v_cvt_pk_bf16_f32 v40, v188, v189\n\t"
        "v_cvt_pk_bf16_f32 v41, v190, v191\n\t"
        "ds_write_b64 v217, v[40:41]\n\t"
        "s_nop 1\n\t"
        "v_cvt_pk_bf16_f32 v42, v192, v193\n\t"
        "v_cvt_pk_bf16_f32 v43, v194, v195\n\t"
        "ds_write_b64 v218, v[42:43]\n\t"
        "ds_read_b128 v[140:143], v219 offset:0\n\t"
        "ds_read_b128 v[144:147], v219 offset:1024\n\t"
        "s_waitcnt lgkmcnt(0)\n\t"
        "s_sub_u32 s71, s74, s72\n\t"
        "s_cmp_gt_u32 s71, 500\n\t"
        "s_cselect_b64 vcc, -1, 0\n\t"
        "v_mfma_f32_16x16x32_bf16 v[24:27], a[0:3], v[0:3], v[44:47]\n\t"
        "v_add_u32 v196, 49152, v212\n\t"
        "v_add_u32 v197, 49152, v213\n\t"
        "v_mfma_f32_16x16x32_bf16 v[28:31], a[4:7], v[0:3], v[48:51]\n\t"
        "ds_read_b128 v[172:175], v196 offset:0\n\t"
        "v_mfma_f32_16x16x32_bf16 v[24:27], a[48:51], v[4:7], v[24:27]\n\t"
        "ds_read_b128 v[176:179], v197 offset:0\n\t"
        "v_mfma_f32_16x16x32_bf16 v[28:31], a[52:55], v[4:7], v[28:31]\n\t"
        "ds_read_b128 v[180:183], v196 offset:128\n\t"
        "v_mfma_f32_16x16x32_bf16 v[24:27], a[96:99], v[8:11], v[24:27]\n\t"
        "ds_read_b128 v[184:187], v197 offset:128\n\t"
        "v_mfma_f32_16x16x32_bf16 v[28:31], a[100:103], v[8:11], v[28:31]\n\t"
        "ds_read_b128 v[188:191], v196 offset:256\n\t"
        "v_mfma_f32_16x16x32_bf16 v[24:27], a[144:147], v[12:15], v[24:27]\n\t"
        "ds_read_b128 v[192:195], v197 offset:256\n\t"
        "v_mfma_f32_16x16x32_bf16 v[28:31], a[148:151], v[12:15], v[28:31]\n\t"
        "v_mfma_f32_16x16x32_bf16 v[24:27], a[192:195], v[16:19], v[24:27]\n\t"
        "v_mfma_f32_16x16x32_bf16 v[28:31], a[196:199], v[16:19], v[28:31]\n\t"
        "v_mfma_f32_16x16x32_bf16 v[24:27], a[240:243], v[20:23], v[24:27]\n\t"
        "s_waitcnt lgkmcnt(0)\n\t"
        "buffer_load_dwordx4 %[xo4], %[rx], s64 offen offset:1024 nt lds\n\t"
        "v_mfma_f32_16x16x32_bf16 v[28:31], a[244:247], v[20:23], v[28:31]\n\t"
        "s_cbranch_vccnz .Lsk1_11\n\t"
        "s_nop 15\n\t"
        "s_nop 15\n\t"
        "s_nop 1\n\t"
        ".Lsk1_11:\n\t"
        "v_mfma_f32_16x16x32_bf16 v[32:35], a[8:11], v[0:3], v[52:55]\n\t"
        "v_mfma_f32_16x16x32_bf16 v[36:39], a[12:15], v[0:3], v[56:59]\n\t"
        "v_mfma_f32_16x16x32_bf16 v[32:35], a[56:59], v[4:7], v[32:35]\n\t"
        "v_mfma_f32_16x16x32_bf16 v[36:39], a[60:63], v[4:7], v[36:39]\n\t"
        "v_mfma_f32_16x16x32_bf16 v[32:35], a[104:107], v[8:11], v[32:35]\n\t"
        "v_mfma_f32_16x16x32_bf16 v[36:39], a[108:111], v[8:11], v[36:39]\n\t"
        "buffer_load_dwordx4 %[xo5], %[rx], s64 offen offset:2048 nt lds\n\t"
        "s_add_u32 s67, s67, 2039808\n\t"
        "s_sub_u32 s71, s67, %[ts]\n\t"
        "s_cselect_b32 s67, s67, s71\n\t"
        "s_add_u32 s68, s68, %[ts]\n\t"
        "s_add_u32 s64, s68, s67\n\t"
        "v_mfma_f32_16x16x32_bf16 v[32:35], a[152:155], v[12:15], v[32:35]\n\t"
        "v_cvt_pk_bf16_f32 v40, v24, v25\n\t"
        "v_mfma_f32_16x16x32_bf16 v[36:39], a[156:159], v[12:15], v[36:39]\n\t"
        "v_cvt_pk_bf16_f32 v41, v26, v27\n\t"
        "v_mfma_f32_16x16x32_bf16 v[32:35], a[200:203], v[16:19], v[32:35]\n\t"
        "v_cvt_pk_bf16_f32 v42, v28, v29\n\t"
        "v_mfma_f32_16x16x32_bf16 v[36:39], a[204:207], v[16:19], v[36:39]\n\t"
        "ds_write_b64 v215, v[40:41]\n\t"
        "v_mfma_f32_16x16x32_bf16 v[32:35], a[248:251], v[20:23], v[32:35]\n\t"
        "v_cvt_pk_bf16_f32 v43, v30, v31\n\t"
        "v_mfma_f32_16x16x32_bf16 v[36:39], a[252:255], v[20:23], v[36:39]\n\t"
        "ds_write_b64 v216, v[42:43]\n\t"
        "s_cbranch_vccnz .Lsk1_23\n\t"
        "s_nop 15\n\t"
        "s_nop 15\n\t"
        "s_nop 1\n\t"
        ".Lsk1_23:\n\t"
        "v_mfma_f32_16x16x32_bf16 v[24:27], a[16:19], v[0:3], v[60:63]\n\t"
        "v_mfma_f32_16x16x32_bf16 v[28:31], a[20:23], v[0:3], v[64:67]\n\t"
        "s_add_u32 m0, %[sl0], 24576\n\t"
        "s_nop 0\n\t"
        "buffer_load_dwordx4 %[xo0], %[rx], s64 offen nt lds\n\t"
        "v_mfma_f32_16x16x32_bf16 v[24:27], a[64:67], v[4:7], v[24:27]\n\t"
        "v_mfma_f32_16x16x32_bf16 v[28:31], a[68:71], v[4:7], v[28:31]\n\t"
        "v_mfma_f32_16x16x32_bf16 v[24:27], a[112:115], v[8:11], v[24:27]\n\t"
        "v_mfma_f32_16x16x32_bf16 v[28:31], a[116:119], v[8:11], v[28:31]\n\t"
        "v_mfma_f32_16x16x32_bf16 v[24:27], a[160:163], v[12:15], v[24:27]\n\t"
        "v_cvt_pk_bf16_f32 v40, v32, v33\n\t"
        "v_mfma_f32_16x16x32_bf16 v[28:31], a[164:167], v[12:15], v[28:31]\n\t"
        "v_cvt_pk_bf16_f32 v41, v34, v35\n\t"
        "s_memrealtime s[74:75]\n\t"
        "v_mfma_f32_16x16x32_bf16 v[24:27], a[208:211], v[16:19], v[24:27]\n\t"
        "v_cvt_pk_bf16_f32 v42, v36, v37\n\t"
        "buffer_load_dwordx4 %[xo1], %[rx], s64 offen offset:1024 nt lds\n\t"
        "v_mfma_f32_16x16x32_bf16 v[28:31], a[212:215], v[16:19], v[28:31]\n\t"
        "ds_write_b64 v217, v[40:41]\n\t"
        "v_mfma_f32_16x16x32_bf16 v[24:27], v[92:95], v[20:23], v[24:27]\n\t"
        "v_cvt_pk_bf16_f32 v43, v38, v39\n\t"
        "v_mfma_f32_16x16x32_bf16 v[28:31], v[96:99], v[20:23], v[28:31]\n\t"
        "ds_write_b64 v218, v[42:43]\n\t"
        "s_waitcnt lgkmcnt(0)\n\t"
        "s_sub_u32 s71, s74, s72\n\t"
        "s_cmp_gt_u32 s71, 547\n\t"
        "s_cselect_b64 vcc, -1, 0\n\t"
        "s_cbranch_vccnz .Lsk1_35\n\t"
        "s_nop 15\n\t"
        "s_nop 15\n\t"
        "s_nop 1\n\t"
        ".Lsk1_35:\n\t"
        "v_mfma_f32_16x16x32_bf16 v[32:35], a[24:27], v[0:3], v[68:71]\n\t"
        "ds_read_b128 v[148:151], v219 offset:0\n\t"
        "v_mfma_f32_16x16x32_bf16 v[36:39], a[28:31], v[0:3], v[72:75]\n\t"
        "ds_read_b128 v[152:155], v219 offset:1024\n\t"
        "v_mfma_f32_16x16x32_bf16 v[32:35], a[72:75], v[4:7], v[32:35]\n\t"
        "v_mfma_f32_16x16x32_bf16 v[36:39], a[76:79], v[4:7], v[36:39]\n\t"
        "v_mfma_f32_16x16x32_bf16 v[32:35], a[120:123], v[8:11], v[32:35]\n\t"
        "buffer_load_dwordx4 %[xo2], %[rx], s64 offen offset:2048 nt lds\n\t"
        "v_mfma_f32_16x16x32_bf16 v[36:39], a[124:127], v[8:11], v[36:39]\n\t"
        "v_mfma_f32_16x16x32_bf16 v[32:35], a[168:171], v[12:15], v[32:35]\n\t"
        "v_cvt_pk_bf16_f32 v40, v24, v25\n\t"
        "v_mfma_f32_16x16x32_bf16 v[36:39], a[172:175], v[12:15], v[36:39]\n\t"
        "v_cvt_pk_bf16_f32 v41, v26, v27\n\t"
        "v_mfma_f32_16x16x32_bf16 v[32:35], a[216:219], v[16:19], v[32:35]\n\t"
        "v_cvt_pk_bf16_f32 v42, v28, v29\n\t"
        "v_mfma_f32_16x16x32_bf16 v[36:39], a[220:223], v[16:19], v[36:39]\n\t"
        "ds_write_b64 v215, v[40:41]\n\t"
        "v_mfma_f32_16x16x32_bf16 v[32:35], v[100:103], v[20:23], v[32:35]\n\t"
        "v_cvt_pk_bf16_f32 v43, v30, v31\n\t"
        "v_mfma_f32_16x16x32_bf16 v[36:39], v[104:107], v[20:23], v[36:39]\n\t"
        "ds_write_b64 v216, v[42:43]\n\t"
        "s_add_u32 m0, %[sl0], 27648\n\t"
        "s_nop 0\n\t"
        "buffer_load_dwordx4 %[xo3], %[rx], s64 offen nt lds\n\t"
        "s_cbranch_vccnz .Lsk1_47\n\t"
        "s_nop 15\n\t"
        "s_nop 15\n\t"
        "s_nop 1\n\t"
        ".Lsk1_47:\n\t"
        "v_mfma_f32_16x16x32_bf16 v[24:27], a[32:35], v[0:3], v[76:79]\n\t"
        "v_mfma_f32_16x16x32_bf16 v[28:31], a[36:39], v[0:3], v[80:83]\n\t"
        "v_mfma_f32_16x16x32_bf16 v[24:27], a[80:83], v[4:7], v[24:27]\n\t"
        "v_mfma_f32_16x16x32_bf16 v[28:31], a[84:87], v[4:7], v[28:31]\n\t"
        "v_mfma_f32_16x16x32_bf16 v[24:27], a[128:131], v[8:11], v[24:27]\n\t"
        "v_mfma_f32_16x16x32_bf16 v[28:31], a[132:135], v[8:11], v[28:31]\n\t"
        "v_mfma_f32_16x16x32_bf16 v[24:27], a[176:179], v[12:15], v[24:27]\n\t"
        "v_cvt_pk_bf16_f32 v40, v32, v33\n\t"
        "v_mfma_f32_16x16x32_bf16 v[28:31], a[180:183], v[12:15], v[28:31]\n\t"
        "v_cvt_pk_bf16_f32 v41, v34, v35\n\t"
        "buffer_load_dwordx4 %[xo4], %[rx], s64 offen offset:1024 nt lds\n\t"
        "v_mfma_f32_16x16x32_bf16 v[24:27], a[224:227], v[16:19], v[24:27]\n\t"
        "v_cvt_pk_bf16_f32 v42, v36, v37\n\t"
        "s_memrealtime s[74:75]\n\t"
        "v_mfma_f32_16x16x32_bf16 v[28:31], a[228:231], v[16:19], v[28:31]\n\t"
        "ds_write_b64 v217, v[40:41]\n\t"
        "v_mfma_f32_16x16x32_bf16 v[24:27], v[108:111], v[20:23], v[24:27]\n\t"
        "v_cvt_pk_bf16_f32 v43, v38, v39\n\t"
        "v_mfma_f32_16x16x32_bf16 v[28:31], v[112:115], v[20:23], v[28:31]\n\t"
        "ds_write_b64 v218, v[42:43]\n\t"
        "s_waitcnt lgkmcnt(0)\n\t"
        "s_sub_u32 s71, s74, s72\n\t"
        "s_cmp_gt_u32 s71, 585\n\t"
        "s_cselect_b64 vcc, -1, 0\n\t"
        "s_cbranch_vccnz .Lsk1_59\n\t"
        "s_nop 15\n\t"
        "s_nop 15\n\t"
        "s_nop 1\n\t"
        ".Lsk1_59:\n\t"
        "v_mfma_f32_16x16x32_bf16 v[32:35], a[40:43], v[0:3], v[84:87]\n\t"
        "ds_read_b128 v[156:159], v219 offset:0\n\t"
        "v_mfma_f32_16x16x32_bf16 v[36:39], a[44:47], v[0:3], v[88:91]\n\t"
        "ds_read_b128 v[160:163], v219 offset:1024\n\t"
        "v_mfma_f32_16x16x32_bf16 v[32:35], a[88:91], v[4:7], v[32:35]\n\t"
        "buffer_load_dwordx4 %[xo5], %[rx], s64 offen offset:2048 nt lds\n\t"
        "s_add_u32 s67, s67, 2039808\n\t"
        "s_sub_u32 s71, s67, %[ts]\n\t"
        "s_cselect_b32 s67, s67, s71\n\t"
        "s_add_u32 s68, s68, %[ts]\n\t"
        "s_add_u32 s64, s68, s67\n\t"
        "v_mfma_f32_16x16x32_bf16 v[36:39], a[92:95], v[4:7], v[36:39]\n\t"
        "v_mfma_f32_16x16x32_bf16 v[32:35], a[136:139], v[8:11], v[32:35]\n\t"
        "v_mfma_f32_16x16x32_bf16 v[36:39], a[140:143], v[8:11], v[36:39]\n\t"
        "v_mfma_f32_16x16x32_bf16 v[32:35], a[184:187], v[12:15], v[32:35]\n\t"
        "v_cvt_pk_bf16_f32 v40, v24, v25\n\t"
        "v_mfma_f32_16x16x32_bf16 v[36:39], a[188:191], v[12:15], v[36:39]\n\t"
        "v_cvt_pk_bf16_f32 v41, v26, v27\n\t"
        "v_mfma_f32_16x16x32_bf16 v[32:35], a[232:235], v[16:19], v[32:35]\n\t"
        "v_cvt_pk_bf16_f32 v42, v28, v29\n\t"
        "v_mfma_f32_16x16x32_bf16 v[36:39], a[236:239], v[16:19], v[36:39]\n\t"
        "ds_write_b64 v215, v[40:41]\n\t"
        "v_mfma_f32_16x16x32_bf16 v[32:35], v[116:119], v[20:23], v[32:35]\n\t"
        "v_cvt_pk_bf16_f32 v43, v30, v31\n\t"
        "v_mfma_f32_16x16x32_bf16 v[36:39], v[120:123], v[20:23], v[36:39]\n\t"
        "ds_write_b64 v216, v[42:43]\n\t"
        "s_cbranch_vccnz .Lsk1_71\n\t"
        "s_nop 15\n\t"
        "s_nop 15\n\t"
        "s_nop 1\n\t"
        ".Lsk1_71:\n\t"
        "v_mfma_f32_16x16x32_bf16 v[24:27], a[0:3], v[172:175], v[44:47]\n\t"
        "v_mfma_f32_16x16x32_bf16 v[28:31], a[4:7], v[172:175], v[48:51]\n\t"
        "s_add_u32 m0, %[sl0], 49152\n\t"
        "s_nop 0\n\t"
        "buffer_load_dwordx4 %[xo0], %[rx], s64 offen nt lds\n\t"
        "v_mfma_f32_16x16x32_bf16 v[24:27], a[48:51], v[176:179], v[24:27]\n\t"
        "v_mfma_f32_16x16x32_bf16 v[28:31], a[52:55], v[176:179], v[28:31]\n\t"
        "v_mfma_f32_16x16x32_bf16 v[24:27], a[96:99], v[180:183], v[24:27]\n\t"
        "v_mfma_f32_16x16x32_bf16 v[28:31], a[100:103], v[180:183], v[28:31]\n\t"
        "v_mfma_f32_16x16x32_bf16 v[24:27], a[144:147], v[184:187], v[24:27]\n\t"
        "v_cvt_pk_bf16_f32 v40, v32, v33\n\t"
        "v_mfma_f32_16x16x32_bf16 v[28:31], a[148:151], v[184:187], v[28:31]\n\t"
        "v_cvt_pk_bf16_f32 v41, v34, v35\n\t"
        "v_mfma_f32_16x16x32_bf16 v[24:27], a[192:195], v[188:191], v[24:27]\n\t"
        "v_cvt_pk_bf16_f32 v42, v36, v37\n\t"
        "v_mfma_f32_16x16x32_bf16 v[28:31], a[196:199], v[188:191], v[28:31]\n\t"
        "ds_write_b64 v217, v[40:41]\n\t"
        "v_mfma_f32_16x16x32_bf16 v[24:27], a[240:243], v[192:195], v[24:27]\n\t"
        "v_cvt_pk_bf16_f32 v43, v38, v39\n\t"
        "v_mfma_f32_16x16x32_bf16 v[28:31], a[244:247], v[192:195], v[28:31]\n\t"
        "ds_write_b64 v218, v[42:43]\n\t"
        "s_cbranch_vccnz .Lsk2_11\n\t"
        "s_nop 15\n\t"
        "s_nop 15\n\t"
        "s_nop 1\n\t"
        ".Lsk2_11:\n\t"
        "v_mfma_f32_16x16x32_bf16 v[32:35], a[8:11], v[172:175], v[52:55]\n\t"
        "ds_read_b128 v[164:167], v219 offset:0\n\t"
        "buffer_load_dwordx4 %[xo1], %[rx], s64 offen offset:1024 nt lds\n\t"
        "v_mfma_f32_16x16x32_bf16 v[36:39], a[12:15], v[172:175], v[56:59]\n\t"
        "ds_read_b128 v[168:171], v219 offset:1024\n\t"
        "v_mfma_f32_16x16x32_bf16 v[32:35], a[56:59], v[176:179], v[32:35]\n\t"
        "v_mfma_f32_16x16x32_bf16 v[36:39], a[60:63], v[176:179], v[36:39]\n\t"
        "v_mfma_f32_16x16x32_bf16 v[32:35], a[104:107], v[180:183], v[32:35]\n\t"
        "v_mfma_f32_16x16x32_bf16 v[36:39], a[108:111], v[180:183], v[36:39]\n\t"
        "v_mfma_f32_16x16x32_bf16 v[32:35], a[152:155], v[184:187], v[32:35]\n\t"
        "v_cvt_pk_bf16_f32 v40, v24, v25\n\t"
        "v_mfma_f32_16x16x32_bf16 v[36:39], a[156:159], v[184:187], v[36:39]\n\t"
        "v_cvt_pk_bf16_f32 v41, v26, v27\n\t"
        "v_mfma_f32_16x16x32_bf16 v[32:35], a[200:203], v[188:191], v[32:35]\n\t"
        "v_cvt_pk_bf16_f32 v42, v28, v29\n\t"
        "v_mfma_f32_16x16x32_bf16 v[36:39], a[204:207], v[188:191], v[36:39]\n\t"
        "ds_write_b64 v215, v[40:41]\n\t"
        "v_mfma_f32_16x16x32_bf16 v[32:35], a[248:251], v[192:195], v[32:35]\n\t"
        "v_cvt_pk_bf16_f32 v43, v30, v31\n\t"
        "v_mfma_f32_16x16x32_bf16 v[36:39], a[252:255], v[192:195], v[36:39]\n\t"
        "ds_write_b64 v216, v[42:43]\n\t"
        "s_cbranch_vccnz .Lsk2_23\n\t"
        "s_nop 15\n\t"
        "s_nop 15\n\t"
        "s_nop 1\n\t"
        ".Lsk2_23:\n\t"
        "v_mfma_f32_16x16x32_bf16 v[24:27], a[16:19], v[172:175], v[60:63]\n\t"
        "buffer_load_dwordx4 %[xo2], %[rx], s64 offen offset:2048 nt lds\n\t"
        "v_mfma_f32_16x16x32_bf16 v[28:31], a[20:23], v[172:175], v[64:67]\n\t"
        "v_mfma_f32_16x16x32_bf16 v[24:27], a[64:67], v[176:179], v[24:27]\n\t"
        "v_mfma_f32_16x16x32_bf16 v[28:31], a[68:71], v[176:179], v[28:31]\n\t"
        "v_mfma_f32_16x16x32_bf16 v[24:27], a[112:115], v[180:183], v[24:27]\n\t"
        "v_mfma_f32_16x16x32_bf16 v[28:31], a[116:119], v[180:183], v[28:31]\n\t"
        "v_mfma_f32_16x16x32_bf16 v[24:27], a[160:163], v[184:187], v[24:27]\n\t"
        "v_cvt_pk_bf16_f32 v40, v32, v33\n\t"
        "v_mfma_f32_16x16x32_bf16 v[28:31], a[164:167], v[184:187], v[28:31]\n\t"
        "v_cvt_pk_bf16_f32 v41, v34, v35\n\t"
        "s_memrealtime s[74:75]\n\t"
        "v_mfma_f32_16x16x32_bf16 v[24:27], a[208:211], v[188:191], v[24:27]\n\t"
        "v_cvt_pk_bf16_f32 v42, v36, v37\n\t"
        "v_mfma_f32_16x16x32_bf16 v[28:31], a[212:215], v[188:191], v[28:31]\n\t"
        "ds_write_b64 v217, v[40:41]\n\t"
        "v_mfma_f32_16x16x32_bf16 v[24:27], v[92:95], v[192:195], v[24:27]\n\t"
        "v_cvt_pk_bf16_f32 v43, v38, v39\n\t"
        "v_mfma_f32_16x16x32_bf16 v[28:31], v[96:99], v[192:195], v[28:31]\n\t"
        "ds_write_b64 v218, v[42:43]\n\t"
        "s_add_u32 m0, %[sl0], 52224\n\t"
        "s_nop 0\n\t"
        "buffer_load_dwordx4 %[xo3], %[rx], s64 offen nt lds\n\t"
        "s_waitcnt lgkmcnt(0)\n\t"
        "s_sub_u32 s71, s74, s72\n\t"
        "s_cmp_gt_u32 s71, 657\n\t"
        "s_cselect_b64 vcc, -1, 0\n\t"
        "s_cbranch_vccnz .Lsk2_35\n\t"
        "s_nop 15\n\t"
        "s_nop 15\n\t"
        "s_nop 1\n\t"
        ".Lsk2_35:\n\t"
        "v_mfma_f32_16x16x32_bf16 v[32:35], a[24:27], v[172:175], v[68:71]\n\t"
        "ds_read_b128 v[0:3], v219 offset:0\n\t"
        "v_mfma_f32_16x16x32_bf16 v[36:39], a[28:31], v[172:175], v[72:75]\n\t"
        "ds_read_b128 v[4:7], v219 offset:1024\n\t"
        "v_mfma_f32_16x16x32_bf16 v[32:35], a[72:75], v[176:179], v[32:35]\n\t"
        "v_mfma_f32_16x16x32_bf16 v[36:39], a[76:79], v[176:179], v[36:39]\n\t"
        "v_mfma_f32_16x16x32_bf16 v[32:35], a[120:123], v[180:183], v[32:35]\n\t"
        "v_mfma_f32_16x16x32_bf16 v[36:39], a[124:127], v[180:183], v[36:39]\n\t"
        "v_mfma_f32_16x16x32_bf16 v[32:35], a[168:171], v[184:187], v[32:35]\n\t"
        "v_cvt_pk_bf16_f32 v40, v24, v25\n\t"
        "v_mfma_f32_16x16x32_bf16 v[36:39], a[172:175], v[184:187], v[36:39]\n\t"
        "v_cvt_pk_bf16_f32 v41, v26, v27\n\t"
        "v_mfma_f32_16x16x32_bf16 v[32:35], a[216:219], v[188:191], v[32:35]\n\t"
        "v_cvt_pk_bf16_f32 v42, v28, v29\n\t"
        "v_mfma_f32_16x16x32_bf16 v[36:39], a[220:223], v[188:191], v[36:39]\n\t"
        "ds_write_b64 v215, v[40:41]\n\t"
        "v_mfma_f32_16x16x32_bf16 v[32:35], v[100:103], v[192:195], v[32:35]\n\t"
        "v_cvt_pk_bf16_f32 v43, v30, v31\n\t"
        "v_mfma_f32_16x16x32_bf16 v[36:39], v[104:107], v[192:195], v[36:39]\n\t"
        "ds_write_b64 v216, v[42:43]\n\t"
        "buffer_load_dwordx4 %[xo4], %[rx], s64 offen offset:1024 nt lds\n\t"
        "s_cbranch_vccnz .Lsk2_47\n\t"
        "s_nop 15\n\t"
        "s_nop 15\n\t"
        "s_nop 1\n\t"
        ".Lsk2_47:\n\t"
        "v_mfma_f32_16x16x32_bf16 v[24:27], a[32:35], v[172:175], v[76:79]\n\t"
        "v_mfma_f32_16x16x32_bf16 v[28:31], a[36:39], v[172:175], v[80:83]\n\t"
        "v_mfma_f32_16x16x32_bf16 v[24:27], a[80:83], v[176:179], v[24:27]\n\t"
        "v_mfma_f32_16x16x32_bf16 v[28:31], a[84:87], v[176:179], v[28:31]\n\t"
        "v_mfma_f32_16x16x32_bf16 v[24:27], a[128:131], v[180:183], v[24:27]\n\t"
        "v_mfma_f32_16x16x32_bf16 v[28:31], a[132:135], v[180:183], v[28:31]\n\t"
        "v_mfma_f32_16x16x32_bf16 v[24:27], a[176:179], v[184:187], v[24:27]\n\t"
        "v_cvt_pk_bf16_f32 v40, v32, v33\n\t"
        "v_mov_b32 v196, v212\n\t"
        "v_mfma_f32_16x16x32_bf16 v[28:31], a[180:183], v[184:187], v[28:31]\n\t"
        "v_cvt_pk_bf16_f32 v41, v34, v35\n\t"
        "v_mov_b32 v197, v213\n\t"
        "v_mfma_f32_16x16x32_bf16 v[24:27], a[224:227], v[188:191], v[24:27]\n\t"
        "v_cvt_pk_bf16_f32 v42, v36, v37\n\t"
        "s_memrealtime s[74:75]\n\t"
        "v_mfma_f32_16x16x32_bf16 v[28:31], a[228:231], v[188:191], v[28:31]\n\t"
        "ds_write_b64 v217, v[40:41]\n\t"
        "v_mfma_f32_16x16x32_bf16 v[24:27], v[108:111], v[192:195], v[24:27]\n\t"
        "v_cvt_pk_bf16_f32 v43, v38, v39\n\t"
        "buffer_load_dwordx4 %[xo5], %[rx], s64 offen offset:2048 nt lds\n\t"
        "s_add_u32 s67, s67, 2039808\n\t"
        "s_sub_u32 s71, s67, %[ts]\n\t"
        "s_cselect_b32 s67, s67, s71\n\t"
        "s_add_u32 s68, s68, %[ts]\n\t"
        "s_add_u32 s64, s68, s67\n\t"
        "v_mfma_f32_16x16x32_bf16 v[28:31], v[112:115], v[192:195], v[28:31]\n\t"
        "ds_write_b64 v218, v[42:43]\n\t"
        "s_waitcnt lgkmcnt(0)\n\t"
        "s_sub_u32 s71, s74, s72\n\t"
        "s_cmp_gt_u32 s71, 695\n\t"
        "s_cselect_b64 vcc, -1, 0\n\t"
        "s_cbranch_vccnz .Lsk2_59\n\t"
        "s_nop 15\n\t"
        "s_nop 15\n\t"
        "s_nop 1\n\t"
        ".Lsk2_59:\n\t"
        "v_mfma_f32_16x16x32_bf16 v[32:35], a[40:43], v[172:175], v[84:87]\n\t"
        "ds_read_b128 v[8:11], v219 offset:0\n\t"
        "s_waitcnt vmcnt(30)\n\t"
        "v_mfma_f32_16x16x32_bf16 v[36:39], a[44:47], v[172:175], v[88:91]\n\t"
        "ds_read_b128 v[12:15], v219 offset:1024\n\t"
        "ds_read_b128 v[172:175], v196 offset:0\n\t"
        "v_mfma_f32_16x16x32_bf16 v[32:35], a[88:91], v[176:179], v[32:35]\n\t"
        "v_mfma_f32_16x16x32_bf16 v[36:39], a[92:95], v[176:179], v[36:39]\n\t"
        "ds_read_b128 v[176:179], v197 offset:0\n\t"
        "v_mfma_f32_16x16x32_bf16 v[32:35], a[136:139], v[180:183], v[32:35]\n\t"
        "v_mfma_f32_16x16x32_bf16 v[36:39], a[140:143], v[180:183], v[36:39]\n\t"
        "ds_read_b128 v[180:183], v196 offset:128\n\t"
        "v_mfma_f32_16x16x32_bf16 v[32:35], a[184:187], v[184:187], v[32:35]\n\t"
        "v_cvt_pk_bf16_f32 v40, v24, v25\n\t"
        "v_mfma_f32_16x16x32_bf16 v[36:39], a[188:191], v[184:187], v[36:39]\n\t"
        "v_cvt_pk_bf16_f32 v41, v26, v27\n\t"
        "ds_read_b128 v[184:187], v197 offset:128\n\t"
        "v_mfma_f32_16x16x32_bf16 v[32:35], a[232:235], v[188:191], v[32:35]\n\t"
        "v_cvt_pk_bf16_f32 v42, v28, v29\n\t"
        "v_mfma_f32_16x16x32_bf16 v[36:39], a[236:239], v[188:191], v[36:39]\n\t"
        "ds_write_b64 v215, v[40:41]\n\t"
        "ds_read_b128 v[188:191], v196 offset:256\n\t"
        "v_mfma_f32_16x16x32_bf16 v[32:35], v[116:119], v[192:195], v[32:35]\n\t"
        "v_cvt_pk_bf16_f32 v43, v30, v31\n\t"
        "v_mfma_f32_16x16x32_bf16 v[36:39], v[120:123], v[192:195], v[36:39]\n\t"
        "ds_write_b64 v216, v[42:43]\n\t"
        "ds_read_b128 v[192:195], v197 offset:256\n\t"
        "s_cbranch_vccnz .Lsk2_71\n\t"
        "s_nop 15\n\t"
        "s_nop 15\n\t"
        "s_nop 1\n\t"
        ".Lsk2_71:\n\t"
        "s_waitcnt lgkmcnt(7)\n\t"
        "v_mfma_f32_16x16x32_bf16 v[24:27], a[0:3], v[172:175], v[44:47]\n\t"
        "v_mfma_f32_16x16x32_bf16 v[28:31], a[4:7], v[172:175], v[48:51]\n\t"
        "s_waitcnt lgkmcnt(6)\n\t"
        "v_mfma_f32_16x16x32_bf16 v[24:27], a[48:51], v[176:179], v[24:27]\n\t"
        "v_mfma_f32_16x16x32_bf16 v[28:31], a[52:55], v[176:179], v[28:31]\n\t"
        "s_waitcnt lgkmcnt(5)\n\t"
        "v_mfma_f32_16x16x32_bf16 v[24:27], a[96:99], v[180:183], v[24:27]\n\t"
        "v_mfma_f32_16x16x32_bf16 v[28:31], a[100:103], v[180:183], v[28:31]\n\t"
        "s_waitcnt lgkmcnt(4)\n\t"
        "v_mfma_f32_16x16x32_bf16 v[24:27], a[144:147], v[184:187], v[24:27]\n\t"
        "v_cvt_pk_bf16_f32 v40, v32, v33\n\t"
        "v_mfma_f32_16x16x32_bf16 v[28:31], a[148:151], v[184:187], v[28:31]\n\t"
        "v_cvt_pk_bf16_f32 v41, v34, v35\n\t"
        "s_waitcnt lgkmcnt(2)\n\t"
        "v_mfma_f32_16x16x32_bf16 v[24:27], a[192:195], v[188:191], v[24:27]\n\t"
        "v_cvt_pk_bf16_f32 v42, v36, v37\n\t"
        "v_mfma_f32_16x16x32_bf16 v[28:31], a[196:199], v[188:191], v[28:31]\n\t"
        "ds_write_b64 v217, v[40:41]\n\t"
        "s_waitcnt lgkmcnt(1)\n\t"
        "v_mfma_f32_16x16x32_bf16 v[24:27], a[240:243], v[192:195], v[24:27]\n\t"
        "v_cvt_pk_bf16_f32 v43, v38, v39\n\t"
        "v_mfma_f32_16x16x32_bf16 v[28:31], a[244:247], v[192:195], v[28:31]\n\t"
        "ds_write_b64 v218, v[42:43]\n\t"
        "s_cbranch_vccnz .Lsk3_11\n\t"
        "s_nop 15\n\t"
        "s_nop 15\n\t"
        "s_nop 1\n\t"
        ".Lsk3_11:\n\t"
        "v_mfma_f32_16x16x32_bf16 v[32:35], a[8:11], v[172:175], v[52:55]\n\t"
        "ds_read_b128 v[16:19], v219 offset:0\n\t"
        "v_mfma_f32_16x16x32_bf16 v[36:39], a[12:15], v[172:175], v[56:59]\n\t"
        "ds_read_b128 v[20:23], v219 offset:1024\n\t"
        "v_mfma_f32_16x16x32_bf16 v[32:35], a[56:59], v[176:179], v[32:35]\n\t"
        "v_mov_b32 v198, v208\n\t"
        "v_mfma_f32_16x16x32_bf16 v[36:39], a[60:63], v[176:179], v[36:39]\n\t"
        "v_mov_b32 v199, v209\n\t"
        "v_mfma_f32_16x16x32_bf16 v[32:35], a[104:107], v[180:183], v[32:35]\n\t"
        "v_mfma_f32_16x16x32_bf16 v[36:39], a[108:111], v[180:183], v[36:39]\n\t"
        "v_mfma_f32_16x16x32_bf16 v[32:35], a[152:155], v[184:187], v[32:35]\n\t"
        "v_cvt_pk_bf16_f32 v40, v24, v25\n\t"
        "v_mov_b32 v200, v210\n\t"
        "v_mfma_f32_16x16x32_bf16 v[36:39], a[156:159], v[184:187], v[36:39]\n\t"
        "v_cvt_pk_bf16_f32 v41, v26, v27\n\t"
        "v_mov_b32 v201, v211\n\t"
        "v_mfma_f32_16x16x32_bf16 v[32:35], a[200:203], v[188:191], v[32:35]\n\t"
        "v_cvt_pk_bf16_f32 v42, v28, v29\n\t"
        "v_mfma_f32_16x16x32_bf16 v[36:39], a[204:207], v[188:191], v[36:39]\n\t"
        "ds_write_b64 v198, v[40:41] offset:0\n\t"
        "v_mfma_f32_16x16x32_bf16 v[32:35], a[248:251], v[192:195], v[32:35]\n\t"
        "v_cvt_pk_bf16_f32 v43, v30, v31\n\t"
        "v_mfma_f32_16x16x32_bf16 v[36:39], a[252:255], v[192:195], v[36:39]\n\t"
        "ds_write_b64 v199, v[42:43] offset:0\n\t"
        "s_cbranch_vccnz .Lsk3_23\n\t"
        "s_nop 15\n\t"
        "s_nop 15\n\t"
        "s_nop 1\n\t"
        ".Lsk3_23:\n\t"
        "v_mfma_f32_16x16x32_bf16 v[24:27], a[16:19], v[172:175], v[60:63]\n\t"
        "v_mfma_f32_16x16x32_bf16 v[28:31], a[20:23], v[172:175], v[64:67]\n\t"
        "v_mfma_f32_16x16x32_bf16 v[24:27], a[64:67], v[176:179], v[24:27]\n\t"
        "s_waitcnt vmcnt(0)\n\t"
        "buffer_store_dwordx4 v[124:127], v220, s[60:63], s66 offen offset:0 nt\n\t"
        "v_mfma_f32_16x16x32_bf16 v[28:31], a[68:71], v[176:179], v[28:31]\n\t"
        "v_mfma_f32_16x16x32_bf16 v[24:27], a[112:115], v[180:183], v[24:27]\n\t"
        "v_mfma_f32_16x16x32_bf16 v[28:31], a[116:119], v[180:183], v[28:31]\n\t"
        "buffer_store_dwordx4 v[128:131], v221, s[60:63], s66 offen offset:0 nt\n\t"
        "v_mfma_f32_16x16x32_bf16 v[24:27], a[160:163], v[184:187], v[24:27]\n\t"
        "v_cvt_pk_bf16_f32 v40, v32, v33\n\t"
        "v_mfma_f32_16x16x32_bf16 v[28:31], a[164:167], v[184:187], v[28:31]\n\t"
        "v_cvt_pk_bf16_f32 v41, v34, v35\n\t"
        "s_memrealtime s[74:75]\n\t"
        "v_mfma_f32_16x16x32_bf16 v[24:27], a[208:211], v[188:191], v[24:27]\n\t"
        "v_cvt_pk_bf16_f32 v42, v36, v37\n\t"
        "v_mfma_f32_16x16x32_bf16 v[28:31], a[212:215], v[188:191], v[28:31]\n\t"
        "ds_write_b64 v200, v[40:41] offset:0\n\t"
        "v_mfma_f32_16x16x32_bf16 v[24:27], v[92:95], v[192:195], v[24:27]\n\t"
        "v_cvt_pk_bf16_f32 v43, v38, v39\n\t"
        "v_mfma_f32_16x16x32_bf16 v[28:31], v[96:99], v[192:195], v[28:31]\n\t"
        "ds_write_b64 v201, v[42:43] offset:0\n\t"
        "s_waitcnt lgkmcnt(0)\n\t"
        "s_sub_u32 s71, s74, s72\n\t"
        "s_cmp_gt_u32 s71, 765\n\t"
        "s_cselect_b64 vcc, -1, 0\n\t"
        "s_cbranch_vccnz .Lsk3_35\n\t"
        "s_nop 15\n\t"
        "s_nop 15\n\t"
        "s_nop 1\n\t"
        ".Lsk3_35:\n\t"
        "v_mfma_f32_16x16x32_bf16 v[32:35], a[24:27], v[172:175], v[68:71]\n\t"
        "v_mfma_f32_16x16x32_bf16 v[36:39], a[28:31], v[172:175], v[72:75]\n\t"
        "v_mfma_f32_16x16x32_bf16 v[32:35], a[72:75], v[176:179], v[32:35]\n\t"
        "v_mfma_f32_16x16x32_bf16 v[36:39], a[76:79], v[176:179], v[36:39]\n\t"
        "v_mfma_f32_16x16x32_bf16 v[32:35], a[120:123], v[180:183], v[32:35]\n\t"
        "v_mfma_f32_16x16x32_bf16 v[36:39], a[124:127], v[180:183], v[36:39]\n\t"
        "buffer_store_dwordx4 v[132:135], v220, s[60:63], s66 offen offset:128 nt\n\t"
        "v_mfma_f32_16x16x32_bf16 v[32:35], a[168:171], v[184:187], v[32:35]\n\t"
        "v_cvt_pk_bf16_f32 v40, v24, v25\n\t"
        "v_mfma_f32_16x16x32_bf16 v[36:39], a[172:175], v[184:187], v[36:39]\n\t"
        "v_cvt_pk_bf16_f32 v41, v26, v27\n\t"
        "v_mfma_f32_16x16x32_bf16 v[32:35], a[216:219], v[188:191], v[32:35]\n\t"
        "v_cvt_pk_bf16_f32 v42, v28, v29\n\t"
        "v_mfma_f32_16x16x32_bf16 v[36:39], a[220:223], v[188:191], v[36:39]\n\t"
        "ds_write_b64 v198, v[40:41] offset:128\n\t"
        "v_mfma_f32_16x16x32_bf16 v[32:35], v[100:103], v[192:195], v[32:35]\n\t"
        "v_cvt_pk_bf16_f32 v43, v30, v31\n\t"
        "v_mfma_f32_16x16x32_bf16 v[36:39], v[104:107], v[192:195], v[36:39]\n\t"
        "ds_write_b64 v199, v[42:43] offset:128\n\t"
        "s_cbranch_vccnz .Lsk3_47\n\t"
        "s_nop 15\n\t"
        "s_nop 15\n\t"
        "s_nop 1\n\t"
        ".Lsk3_47:\n\t"
        "v_mfma_f32_16x16x32_bf16 v[24:27], a[32:35], v[172:175], v[76:79]\n\t"
        "v_mfma_f32_16x16x32_bf16 v[28:31], a[36:39], v[172:175], v[80:83]\n\t"
        "v_mfma_f32_16x16x32_bf16 v[24:27], a[80:83], v[176:179], v[24:27]\n\t"
        "v_mfma_f32_16x16x32_bf16 v[28:31], a[84:87], v[176:179], v[28:31]\n\t"
        "v_mfma_f32_16x16x32_bf16 v[24:27], a[128:131], v[180:183], v[24:27]\n\t"
        "v_mfma_f32_16x16x32_bf16 v[28:31], a[132:135], v[180:183], v[28:31]\n\t"
        "buffer_store_dwordx4 v[136:139], v221, s[60:63], s66 offen offset:128 nt\n\t"
        "v_mfma_f32_16x16x32_bf16 v[24:27], a[176:179], v[184:187], v[24:27]\n\t"
        "v_cvt_pk_bf16_f32 v40, v32, v33\n\t"
        "v_add_u32 v196, 73728, v212\n\t"
        "v_mfma_f32_16x16x32_bf16 v[28:31], a[180:183], v[184:187], v[28:31]\n\t"
        "v_cvt_pk_bf16_f32 v41, v34, v35\n\t"
        "v_add_u32 v197, 73728, v213\n\t"
        "v_mfma_f32_16x16x32_bf16 v[24:27], a[224:227], v[188:191], v[24:27]\n\t"
        "v_cvt_pk_bf16_f32 v42, v36, v37\n\t"
        "s_memrealtime s[74:75]\n\t"
        "v_mfma_f32_16x16x32_bf16 v[28:31], a[228:231], v[188:191], v[28:31]\n\t"
        "ds_write_b64 v200, v[40:41] offset:128\n\t"
        "v_mfma_f32_16x16x32_bf16 v[24:27], v[108:111], v[192:195], v[24:27]\n\t"
        "v_cvt_pk_bf16_f32 v43, v38, v39\n\t"
        "v_mfma_f32_16x16x32_bf16 v[28:31], v[112:115], v[192:195], v[28:31]\n\t"
        "ds_write_b64 v201, v[42:43] offset:128\n\t"
        "s_waitcnt lgkmcnt(0)\n\t"
        "s_sub_u32 s71, s74, s72\n\t"
        "s_cmp_gt_u32 s71, 801\n\t"
        "s_cselect_b64 vcc, -1, 0\n\t"
        "s_cbranch_vccnz .Lsk3_59\n\t"
        "s_nop 15\n\t"
        "s_nop 15\n\t"
        "s_nop 1\n\t"
        ".Lsk3_59:\n\t"
        "v_mfma_f32_16x16x32_bf16 v[32:35], a[40:43], v[172:175], v[84:87]\n\t"
        "v_mfma_f32_16x16x32_bf16 v[36:39], a[44:47], v[172:175], v[88:91]\n\t"
        "ds_read_b128 v[172:175], v196 offset:0\n\t"
        "v_mfma_f32_16x16x32_bf16 v[32:35], a[88:91], v[176:179], v[32:35]\n\t"
        "v_mfma_f32_16x16x32_bf16 v[36:39], a[92:95], v[176:179], v[36:39]\n\t"
        "ds_read_b128 v[176:179], v197 offset:0\n\t"
        "v_mfma_f32_16x16x32_bf16 v[32:35], a[136:139], v[180:183], v[32:35]\n\t"
        "v_mfma_f32_16x16x32_bf16 v[36:39], a[140:143], v[180:183], v[36:39]\n\t"
        "ds_read_b128 v[180:183], v196 offset:128\n\t"
        "v_mfma_f32_16x16x32_bf16 v[32:35], a[184:187], v[184:187], v[32:35]\n\t"
        "v_cvt_pk_bf16_f32 v40, v24, v25\n\t"
        "v_mfma_f32_16x16x32_bf16 v[36:39], a[188:191], v[184:187], v[36:39]\n\t"
        "v_cvt_pk_bf16_f32 v41, v26, v27\n\t"
        "ds_read_b128 v[184:187], v197 offset:128\n\t"
        "v_mfma_f32_16x16x32_bf16 v[32:35], a[232:235], v[188:191], v[32:35]\n\t"
        "v_cvt_pk_bf16_f32 v42, v28, v29\n\t"
        "v_mfma_f32_16x16x32_bf16 v[36:39], a[236:239], v[188:191], v[36:39]\n\t"
        "ds_write_b64 v198, v[40:41] offset:256\n\t"
        "ds_read_b128 v[188:191], v196 offset:256\n\t"
        "v_mfma_f32_16x16x32_bf16 v[32:35], v[116:119], v[192:195], v[32:35]\n\t"
        "v_cvt_pk_bf16_f32 v43, v30, v31\n\t"
        "v_mfma_f32_16x16x32_bf16 v[36:39], v[120:123], v[192:195], v[36:39]\n\t"
        "ds_write_b64 v199, v[42:43] offset:256\n\t"
        "ds_read_b128 v[192:195], v197 offset:256\n\t"
        "s_cbranch_vccnz .Lsk3_71\n\t"
        "s_nop 15\n\t"
        "s_nop 15\n\t"
        "s_nop 1\n\t"
        ".Lsk3_71:\n\t"
        "s_waitcnt lgkmcnt(7)\n\t"
        "v_mfma_f32_16x16x32_bf16 v[24:27], a[0:3], v[172:175], v[44:47]\n\t"
        "v_mfma_f32_16x16x32_bf16 v[28:31], a[4:7], v[172:175], v[48:51]\n\t"
        "s_waitcnt lgkmcnt(6)\n\t"
        "v_mfma_f32_16x16x32_bf16 v[24:27], a[48:51], v[176:179], v[24:27]\n\t"
        "v_mfma_f32_16x16x32_bf16 v[28:31], a[52:55], v[176:179], v[28:31]\n\t"
        "buffer_store_dwordx4 v[140:143], v220, s[60:63], s66 offen offset:256 nt\n\t"
        "s_waitcnt lgkmcnt(5)\n\t"
        "v_mfma_f32_16x16x32_bf16 v[24:27], a[96:99], v[180:183], v[24:27]\n\t"
        "v_mfma_f32_16x16x32_bf16 v[28:31], a[100:103], v[180:183], v[28:31]\n\t"
        "buffer_store_dwordx4 v[144:147], v221, s[60:63], s66 offen offset:256 nt\n\t"
        "s_add_u32 s69, s69, 2039808\n\t"
        "s_sub_u32 s71, s69, %[ts]\n\t"
        "s_cselect_b32 s69, s69, s71\n\t"
        "s_add_u32 s70, s70, %[ts]\n\t"
        "s_add_u32 s66, s70, s69\n\t"
        "s_waitcnt lgkmcnt(4)\n\t"
        "v_mfma_f32_16x16x32_bf16 v[24:27], a[144:147], v[184:187], v[24:27]\n\t"
        "v_cvt_pk_bf16_f32 v40, v32, v33\n\t"
        "v_mfma_f32_16x16x32_bf16 v[28:31], a[148:151], v[184:187], v[28:31]\n\t"
        "v_cvt_pk_bf16_f32 v41, v34, v35\n\t"
        "s_waitcnt lgkmcnt(2)\n\t"
        "v_mfma_f32_16x16x32_bf16 v[24:27], a[192:195], v[188:191], v[24:27]\n\t"
        "v_cvt_pk_bf16_f32 v42, v36, v37\n\t"
        "v_mfma_f32_16x16x32_bf16 v[28:31], a[196:199], v[188:191], v[28:31]\n\t"
        "ds_write_b64 v200, v[40:41] offset:256\n\t"
        "s_waitcnt lgkmcnt(1)\n\t"
        "v_mfma_f32_16x16x32_bf16 v[24:27], a[240:243], v[192:195], v[24:27]\n\t"
        "v_cvt_pk_bf16_f32 v43, v38, v39\n\t"
        "v_mfma_f32_16x16x32_bf16 v[28:31], a[244:247], v[192:195], v[28:31]\n\t"
        "ds_write_b64 v201, v[42:43] offset:256\n\t"
        "s_cbranch_vccnz .Lsk4_11\n\t"
        "s_nop 15\n\t"
        "s_nop 15\n\t"
        "s_nop 1\n\t"
        ".Lsk4_11:\n\t"
        "v_mfma_f32_16x16x32_bf16 v[32:35], a[8:11], v[172:175], v[52:55]\n\t"
        "v_mfma_f32_16x16x32_bf16 v[36:39], a[12:15], v[172:175], v[56:59]\n\t"
        "v_mfma_f32_16x16x32_bf16 v[32:35], a[56:59], v[176:179], v[32:35]\n\t"
        "v_add_u32 v198, 73728, v208\n\t"
        "v_mfma_f32_16x16x32_bf16 v[36:39], a[60:63], v[176:179], v[36:39]\n\t"
        "v_add_u32 v199, 73728, v209\n\t"
        "v_mfma_f32_16x16x32_bf16 v[32:35], a[104:107], v[180:183], v[32:35]\n\t"
        "v_mfma_f32_16x16x32_bf16 v[36:39], a[108:111], v[180:183], v[36:39]\n\t"
        "v_mfma_f32_16x16x32_bf16 v[32:35], a[152:155], v[184:187], v[32:35]\n\t"
        "v_cvt_pk_bf16_f32 v40, v24, v25\n\t"
        "v_add_u32 v200, 73728, v210\n\t"
        "v_mfma_f32_16x16x32_bf16 v[36:39], a[156:159], v[184:187], v[36:39]\n\t"
        "v_cvt_pk_bf16_f32 v41, v26, v27\n\t"
        "v_add_u32 v201, 73728, v211\n\t"
        "v_mfma_f32_16x16x32_bf16 v[32:35], a[200:203], v[188:191], v[32:35]\n\t"
        "v_cvt_pk_bf16_f32 v42, v28, v29\n\t"
        "v_mfma_f32_16x16x32_bf16 v[36:39], a[204:207], v[188:191], v[36:39]\n\t"
        "ds_write_b64 v198, v[40:41] offset:0\n\t"
        "v_mfma_f32_16x16x32_bf16 v[32:35], a[248:251], v[192:195], v[32:35]\n\t"
        "v_cvt_pk_bf16_f32 v43, v30, v31\n\t"
        "v_mfma_f32_16x16x32_bf16 v[36:39], a[252:255], v[192:195], v[36:39]\n\t"
        "ds_write_b64 v199, v[42:43] offset:0\n\t"
        "s_cbranch_vccnz .Lsk4_23\n\t"
        "s_nop 15\n\t"
        "s_nop 15\n\t"
        "s_nop 1\n\t"
        ".Lsk4_23:\n\t"
        "v_mfma_f32_16x16x32_bf16 v[24:27], a[16:19], v[172:175], v[60:63]\n\t"
        "v_mfma_f32_16x16x32_bf16 v[28:31], a[20:23], v[172:175], v[64:67]\n\t"
        "v_mfma_f32_16x16x32_bf16 v[24:27], a[64:67], v[176:179], v[24:27]\n\t"
        "buffer_store_dwordx4 v[148:151], v220, s[60:63], s66 offen offset:0 nt\n\t"
        "v_mfma_f32_16x16x32_bf16 v[28:31], a[68:71], v[176:179], v[28:31]\n\t"
        "v_mfma_f32_16x16x32_bf16 v[24:27], a[112:115], v[180:183], v[24:27]\n\t"
        "v_mfma_f32_16x16x32_bf16 v[28:31], a[116:119], v[180:183], v[28:31]\n\t"
        "buffer_store_dwordx4 v[152:155], v221, s[60:63], s66 offen offset:0 nt\n\t"
        "v_mfma_f32_16x16x32_bf16 v[24:27], a[160:163], v[184:187], v[24:27]\n\t"
        "v_cvt_pk_bf16_f32 v40, v32, v33\n\t"
        "v_mfma_f32_16x16x32_bf16 v[28:31], a[164:167], v[184:187], v[28:31]\n\t"
        "v_cvt_pk_bf16_f32 v41, v34, v35\n\t"
        "s_memrealtime s[74:75]\n\t"
        "v_mfma_f32_16x16x32_bf16 v[24:27], a[208:211], v[188:191], v[24:27]\n\t"
        "v_cvt_pk_bf16_f32 v42, v36, v37\n\t"
        "v_mfma_f32_16x16x32_bf16 v[28:31], a[212:215], v[188:191], v[28:31]\n\t"
        "ds_write_b64 v200, v[40:41] offset:0\n\t"
        "v_mfma_f32_16x16x32_bf16 v[24:27], v[92:95], v[192:195], v[24:27]\n\t"
        "v_cvt_pk_bf16_f32 v43, v38, v39\n\t"
        "v_mfma_f32_16x16x32_bf16 v[28:31], v[96:99], v[192:195], v[28:31]\n\t"
        "ds_write_b64 v201, v[42:43] offset:0\n\t"
        "s_waitcnt lgkmcnt(0)\n\t"
        "s_sub_u32 s71, s74, s72\n\t"
        "s_cmp_gt_u32 s71, 871\n\t"
        "s_cselect_b64 vcc, -1, 0\n\t"
        "s_cbranch_vccnz .Lsk4_35\n\t"
        "s_nop 15\n\t"
        "s_nop 15\n\t"
        "s_nop 1\n\t"
        ".Lsk4_35:\n\t"
        "v_mfma_f32_16x16x32_bf16 v[32:35], a[24:27], v[172:175], v[68:71]\n\t"
        "v_mfma_f32_16x16x32_bf16 v[36:39], a[28:31], v[172:175], v[72:75]\n\t"
        "v_mfma_f32_16x16x32_bf16 v[32:35], a[72:75], v[176:179], v[32:35]\n\t"
        "buffer_store_dwordx4 v[156:159], v220, s[60:63], s66 offen offset:128 nt\n\t"
        "v_mfma_f32_16x16x32_bf16 v[36:39], a[76:79], v[176:179], v[36:39]\n\t"
        "v_mfma_f32_16x16x32_bf16 v[32:35], a[120:123], v[180:183], v[32:35]\n\t"
        "v_mfma_f32_16x16x32_bf16 v[36:39], a[124:127], v[180:183], v[36:39]\n\t"
        "buffer_store_dwordx4 v[160:163], v221, s[60:63], s66 offen offset:128 nt\n\t"
        "v_mfma_f32_16x16x32_bf16 v[32:35], a[168:171], v[184:187], v[32:35]\n\t"
        "v_cvt_pk_bf16_f32 v40, v24, v25\n\t"
        "v_mfma_f32_16x16x32_bf16 v[36:39], a[172:175], v[184:187], v[36:39]\n\t"
        "v_cvt_pk_bf16_f32 v41, v26, v27\n\t"
        "v_mfma_f32_16x16x32_bf16 v[32:35], a[216:219], v[188:191], v[32:35]\n\t"
        "v_cvt_pk_bf16_f32 v42, v28, v29\n\t"
        "v_mfma_f32_16x16x32_bf16 v[36:39], a[220:223], v[188:191], v[36:39]\n\t"
        "ds_write_b64 v198, v[40:41] offset:128\n\t"
        "v_mfma_f32_16x16x32_bf16 v[32:35], v[100:103], v[192:195], v[32:35]\n\t"
        "v_cvt_pk_bf16_f32 v43, v30, v31\n\t"
        "v_mfma_f32_16x16x32_bf16 v[36:39], v[104:107], v[192:195], v[36:39]\n\t"
        "ds_write_b64 v199, v[42:43] offset:128\n\t"
        "s_cbranch_vccnz .Lsk4_47\n\t"
        "s_nop 15\n\t"
        "s_nop 15\n\t"
        "s_nop 1\n\t"
        ".Lsk4_47:\n\t"
        "v_mfma_f32_16x16x32_bf16 v[24:27], a[32:35], v[172:175], v[76:79]\n\t"
        "v_mfma_f32_16x16x32_bf16 v[28:31], a[36:39], v[172:175], v[80:83]\n\t"
        "v_mfma_f32_16x16x32_bf16 v[24:27], a[80:83], v[176:179], v[24:27]\n\t"
        "buffer_store_dwordx4 v[164:167], v220, s[60:63], s66 offen offset:256 nt\n\t"
        "v_mfma_f32_16x16x32_bf16 v[28:31], a[84:87], v[176:179], v[28:31]\n\t"
        "v_mfma_f32_16x16x32_bf16 v[24:27], a[128:131], v[180:183], v[24:27]\n\t"
        "v_mfma_f32_16x16x32_bf16 v[28:31], a[132:135], v[180:183], v[28:31]\n\t"
        "buffer_store_dwordx4 v[168:171], v221, s[60:63], s66 offen offset:256 nt\n\t"
        "s_add_u32 s69, s69, 2039808\n\t"
        "s_sub_u32 s71, s69, %[ts]\n\t"
        "s_cselect_b32 s69, s69, s71\n\t"
        "s_add_u32 s70, s70, %[ts]\n\t"
        "s_add_u32 s66, s70, s69\n\t"
        "v_mfma_f32_16x16x32_bf16 v[24:27], a[176:179], v[184:187], v[24:27]\n\t"
        "v_cvt_pk_bf16_f32 v40, v32, v33\n\t"
        "v_add_u32 v196, 98304, v212\n\t"
        "v_mfma_f32_16x16x32_bf16 v[28:31], a[180:183], v[184:187], v[28:31]\n\t"
        "v_cvt_pk_bf16_f32 v41, v34, v35\n\t"
        "v_add_u32 v197, 98304, v213\n\t"
        "v_mfma_f32_16x16x32_bf16 v[24:27], a[224:227], v[188:191], v[24:27]\n\t"
        "v_cvt_pk_bf16_f32 v42, v36, v37\n\t"
        "s_memrealtime s[74:75]\n\t"
        "v_mfma_f32_16x16x32_bf16 v[28:31], a[228:231], v[188:191], v[28:31]\n\t"
        "ds_write_b64 v200, v[40:41] offset:128\n\t"
        "v_mfma_f32_16x16x32_bf16 v[24:27], v[108:111], v[192:195], v[24:27]\n\t"
        "v_cvt_pk_bf16_f32 v43, v38, v39\n\t"
        "v_mfma_f32_16x16x32_bf16 v[28:31], v[112:115], v[192:195], v[28:31]\n\t"
        "ds_write_b64 v201, v[42:43] offset:128\n\t"
        "s_waitcnt lgkmcnt(0)\n\t"
        "s_sub_u32 s71, s74, s72\n\t"
        "s_cmp_gt_u32 s71, 909\n\t"
        "s_cselect_b64 vcc, -1, 0\n\t"
        "s_cbranch_vccnz .Lsk4_59\n\t"
        "s_nop 15\n\t"
        "s_nop 15\n\t"
        "s_nop 1\n\t"
        ".Lsk4_59:\n\t"
        "v_mfma_f32_16x16x32_bf16 v[32:35], a[40:43], v[172:175], v[84:87]\n\t"
        "v_mfma_f32_16x16x32_bf16 v[36:39], a[44:47], v[172:175], v[88:91]\n\t"
        "ds_read_b128 v[172:175], v196 offset:0\n\t"
        "v_mfma_f32_16x16x32_bf16 v[32:35], a[88:91], v[176:179], v[32:35]\n\t"
        "v_mfma_f32_16x16x32_bf16 v[36:39], a[92:95], v[176:179], v[36:39]\n\t"
        "ds_read_b128 v[176:179], v197 offset:0\n\t"
        "v_mfma_f32_16x16x32_bf16 v[32:35], a[136:139], v[180:183], v[32:35]\n\t"
        "v_mfma_f32_16x16x32_bf16 v[36:39], a[140:143], v[180:183], v[36:39]\n\t"
        "ds_read_b128 v[180:183], v196 offset:128\n\t"
        "v_mfma_f32_16x16x32_bf16 v[32:35], a[184:187], v[184:187], v[32:35]\n\t"
        "v_cvt_pk_bf16_f32 v40, v24, v25\n\t"
        "v_mfma_f32_16x16x32_bf16 v[36:39], a[188:191], v[184:187], v[36:39]\n\t"
        "v_cvt_pk_bf16_f32 v41, v26, v27\n\t"
        "ds_read_b128 v[184:187], v197 offset:128\n\t"
        "v_mfma_f32_16x16x32_bf16 v[32:35], a[232:235], v[188:191], v[32:35]\n\t"
        "v_cvt_pk_bf16_f32 v42, v28, v29\n\t"
        "v_mfma_f32_16x16x32_bf16 v[36:39], a[236:239], v[188:191], v[36:39]\n\t"
        "ds_write_b64 v198, v[40:41] offset:256\n\t"
        "ds_read_b128 v[188:191], v196 offset:256\n\t"
        "v_mfma_f32_16x16x32_bf16 v[32:35], v[116:119], v[192:195], v[32:35]\n\t"
        "v_cvt_pk_bf16_f32 v43, v30, v31\n\t"
        "v_mfma_f32_16x16x32_bf16 v[36:39], v[120:123], v[192:195], v[36:39]\n\t"
        "ds_write_b64 v199, v[42:43] offset:256\n\t"
        "ds_read_b128 v[192:195], v197 offset:256\n\t"
        "s_cbranch_vccnz .Lsk4_71\n\t"
        "s_nop 15\n\t"
        "s_nop 15\n\t"
        "s_nop 1\n\t"
        ".Lsk4_71:\n\t"
        "s_waitcnt lgkmcnt(7)\n\t"
        "v_mfma_f32_16x16x32_bf16 v[24:27], a[0:3], v[172:175], v[44:47]\n\t"
        "v_mfma_f32_16x16x32_bf16 v[28:31], a[4:7], v[172:175], v[48:51]\n\t"
        "s_waitcnt lgkmcnt(6)\n\t"
        "v_mfma_f32_16x16x32_bf16 v[24:27], a[48:51], v[176:179], v[24:27]\n\t"
        "v_mov_b32 v202, v214\n\t"
        "v_mfma_f32_16x16x32_bf16 v[28:31], a[52:55], v[176:179], v[28:31]\n\t"
        "buffer_store_dwordx4 v[0:3], v220, s[60:63], s66 offen offset:0 nt\n\t"
        "s_waitcnt lgkmcnt(5)\n\t"
        "v_mfma_f32_16x16x32_bf16 v[24:27], a[96:99], v[180:183], v[24:27]\n\t"
        "v_mfma_f32_16x16x32_bf16 v[28:31], a[100:103], v[180:183], v[28:31]\n\t"
        "buffer_store_dwordx4 v[4:7], v221, s[60:63], s66 offen offset:0 nt\n\t"
        "s_waitcnt lgkmcnt(4)\n\t"
        "v_mfma_f32_16x16x32_bf16 v[24:27], a[144:147], v[184:187], v[24:27]\n\t"
        "v_cvt_pk_bf16_f32 v40, v32, v33\n\t"
        "v_mfma_f32_16x16x32_bf16 v[28:31], a[148:151], v[184:187], v[28:31]\n\t"
        "v_cvt_pk_bf16_f32 v41, v34, v35\n\t"
        "s_waitcnt lgkmcnt(2)\n\t"
        "v_mfma_f32_16x16x32_bf16 v[24:27], a[192:195], v[188:191], v[24:27]\n\t"
        "v_cvt_pk_bf16_f32 v42, v36, v37\n\t"
        "v_mfma_f32_16x16x32_bf16 v[28:31], a[196:199], v[188:191], v[28:31]\n\t"
        "ds_write_b64 v200, v[40:41] offset:256\n\t"
        "s_waitcnt lgkmcnt(1)\n\t"
        "v_mfma_f32_16x16x32_bf16 v[24:27], a[240:243], v[192:195], v[24:27]\n\t"
        "v_cvt_pk_bf16_f32 v43, v38, v39\n\t"
        "v_mfma_f32_16x16x32_bf16 v[28:31], a[244:247], v[192:195], v[28:31]\n\t"
        "ds_write_b64 v201, v[42:43] offset:256\n\t"
        "s_cbranch_vccnz .Lsk5_11\n\t"
        "s_nop 15\n\t"
        "s_nop 15\n\t"
        "s_nop 1\n\t"
        ".Lsk5_11:\n\t"
        "v_mfma_f32_16x16x32_bf16 v[32:35], a[8:11], v[172:175], v[52:55]\n\t"
        "ds_read_b128 v[124:127], v202 offset:0\n\t"
        "v_mfma_f32_16x16x32_bf16 v[36:39], a[12:15], v[172:175], v[56:59]\n\t"
        "ds_read_b128 v[128:131], v202 offset:1024\n\t"
        "v_mfma_f32_16x16x32_bf16 v[32:35], a[56:59], v[176:179], v[32:35]\n\t"
        "v_add_u32 v198, 98304, v208\n\t"
        "ds_read_b128 v[132:135], v202 offset:2048\n\t"
        "v_mfma_f32_16x16x32_bf16 v[36:39], a[60:63], v[176:179], v[36:39]\n\t"
        "v_add_u32 v199, 98304, v209\n\t"
        "ds_read_b128 v[136:139], v202 offset:3072\n\t"
        "v_mfma_f32_16x16x32_bf16 v[32:35], a[104:107], v[180:183], v[32:35]\n\t"
        "ds_read_b128 v[140:143], v202 offset:4096\n\t"
        "v_mfma_f32_16x16x32_bf16 v[36:39], a[108:111], v[180:183], v[36:39]\n\t"
        "ds_read_b128 v[144:147], v202 offset:5120\n\t"
        "v_mfma_f32_16x16x32_bf16 v[32:35], a[152:155], v[184:187], v[32:35]\n\t"
        "v_cvt_pk_bf16_f32 v40, v24, v25\n\t"
        "v_add_u32 v200, 98304, v210\n\t"
        "v_mfma_f32_16x16x32_bf16 v[36:39], a[156:159], v[184:187], v[36:39]\n\t"
        "v_cvt_pk_bf16_f32 v41, v26, v27\n\t"
        "v_add_u32 v201, 98304, v211\n\t"
        "v_mfma_f32_16x16x32_bf16 v[32:35], a[200:203], v[188:191], v[32:35]\n\t"
        "v_cvt_pk_bf16_f32 v42, v28, v29\n\t"
        "v_mfma_f32_16x16x32_bf16 v[36:39], a[204:207], v[188:191], v[36:39]\n\t"
        "ds_write_b64 v198, v[40:41] offset:0\n\t"
        "v_mfma_f32_16x16x32_bf16 v[32:35], a[248:251], v[192:195], v[32:35]\n\t"
        "v_cvt_pk_bf16_f32 v43, v30, v31\n\t"
        "v_mfma_f32_16x16x32_bf16 v[36:39], a[252:255], v[192:195], v[36:39]\n\t"
        "ds_write_b64 v199, v[42:43] offset:0\n\t"
        "s_cbranch_vccnz .Lsk5_23\n\t"
        "s_nop 15\n\t"
        "s_nop 15\n\t"
        "s_nop 1\n\t"
        ".Lsk5_23:\n\t"
        "v_mfma_f32_16x16x32_bf16 v[24:27], a[16:19], v[172:175], v[60:63]\n\t"
        "v_mfma_f32_16x16x32_bf16 v[28:31], a[20:23], v[172:175], v[64:67]\n\t"
        "v_mfma_f32_16x16x32_bf16 v[24:27], a[64:67], v[176:179], v[24:27]\n\t"
        "buffer_store_dwordx4 v[8:11], v220, s[60:63], s66 offen offset:128 nt\n\t"
        "v_mfma_f32_16x16x32_bf16 v[28:31], a[68:71], v[176:179], v[28:31]\n\t"
        "v_mfma_f32_16x16x32_bf16 v[24:27], a[112:115], v[180:183], v[24:27]\n\t"
        "v_mfma_f32_16x16x32_bf16 v[28:31], a[116:119], v[180:183], v[28:31]\n\t"
        "buffer_store_dwordx4 v[12:15], v221, s[60:63], s66 offen offset:128 nt\n\t"
        "v_mfma_f32_16x16x32_bf16 v[24:27], a[160:163], v[184:187], v[24:27]\n\t"
        "v_cvt_pk_bf16_f32 v40, v32, v33\n\t"
        "v_mfma_f32_16x16x32_bf16 v[28:31], a[164:167], v[184:187], v[28:31]\n\t"
        "v_cvt_pk_bf16_f32 v41, v34, v35\n\t"
        "s_memrealtime s[74:75]\n\t"
        "v_mfma_f32_16x16x32_bf16 v[24:27], a[208:211], v[188:191], v[24:27]\n\t"
        "v_cvt_pk_bf16_f32 v42, v36, v37\n\t"
        "v_mfma_f32_16x16x32_bf16 v[28:31], a[212:215], v[188:191], v[28:31]\n\t"
        "ds_write_b64 v200, v[40:41] offset:0\n\t"
        "v_mfma_f32_16x16x32_bf16 v[24:27], v[92:95], v[192:195], v[24:27]\n\t"
        "v_cvt_pk_bf16_f32 v43, v38, v39\n\t"
        "v_mfma_f32_16x16x32_bf16 v[28:31], v[96:99], v[192:195], v[28:31]\n\t"
        "ds_write_b64 v201, v[42:43] offset:0\n\t"
        "s_waitcnt lgkmcnt(0)\n\t"
        "s_sub_u32 s71, s74, s72\n\t"
        "s_cmp_gt_u32 s71, 979\n\t"
        "s_cselect_b64 vcc, -1, 0\n\t"
        "s_cbranch_vccnz .Lsk5_35\n\t"
        "s_nop 15\n\t"
        "s_nop 15\n\t"
        "s_nop 1\n\t"
        ".Lsk5_35:\n\t"
        "v_mfma_f32_16x16x32_bf16 v[32:35], a[24:27], v[172:175], v[68:71]\n\t"
        "v_mfma_f32_16x16x32_bf16 v[36:39], a[28:31], v[172:175], v[72:75]\n\t"
        "v_mfma_f32_16x16x32_bf16 v[32:35], a[72:75], v[176:179], v[32:35]\n\t"
        "buffer_store_dwordx4 v[16:19], v220, s[60:63], s66 offen offset:256 nt\n\t"
        "v_mfma_f32_16x16x32_bf16 v[36:39], a[76:79], v[176:179], v[36:39]\n\t"
        "v_mfma_f32_16x16x32_bf16 v[32:35], a[120:123], v[180:183], v[32:35]\n\t"
        "v_mfma_f32_16x16x32_bf16 v[36:39], a[124:127], v[180:183], v[36:39]\n\t"
        "buffer_store_dwordx4 v[20:23], v221, s[60:63], s66 offen offset:256 nt\n\t"
        "s_add_u32 s69, s69, 2039808\n\t"
        "s_sub_u32 s71, s69, %[ts]\n\t"
        "s_cselect_b32 s69, s69, s71\n\t"
        "s_add_u32 s70, s70, %[ts]\n\t"
        "s_add_u32 s66, s70, s69\n\t"
        "v_mfma_f32_16x16x32_bf16 v[32:35], a[168:171], v[184:187], v[32:35]\n\t"
        "v_cvt_pk_bf16_f32 v40, v24, v25\n\t"
        "v_mfma_f32_16x16x32_bf16 v[36:39], a[172:175], v[184:187], v[36:39]\n\t"
        "v_cvt_pk_bf16_f32 v41, v26, v27\n\t"
        "v_mfma_f32_16x16x32_bf16 v[32:35], a[216:219], v[188:191], v[32:35]\n\t"
        "v_cvt_pk_bf16_f32 v42, v28, v29\n\t"
        "v_mfma_f32_16x16x32_bf16 v[36:39], a[220:223], v[188:191], v[36:39]\n\t"
        "ds_write_b64 v198, v[40:41] offset:128\n\t"
        "v_mfma_f32_16x16x32_bf16 v[32:35], v[100:103], v[192:195], v[32:35]\n\t"
        "v_cvt_pk_bf16_f32 v43, v30, v31\n\t"
        "v_mfma_f32_16x16x32_bf16 v[36:39], v[104:107], v[192:195], v[36:39]\n\t"
        "ds_write_b64 v199, v[42:43] offset:128\n\t"
        "s_cbranch_vccnz .Lsk5_47\n\t"
        "s_nop 15\n\t"
        "s_nop 15\n\t"
        "s_nop 1\n\t"
        ".Lsk5_47:\n\t"
        "v_mfma_f32_16x16x32_bf16 v[24:27], a[32:35], v[172:175], v[76:79]\n\t"
        "v_mfma_f32_16x16x32_bf16 v[28:31], a[36:39], v[172:175], v[80:83]\n\t"
        "v_mfma_f32_16x16x32_bf16 v[24:27], a[80:83], v[176:179], v[24:27]\n\t"
        "buffer_store_dwordx4 v[124:127], %[xo0], s[60:63], s66 offen nt\n\t"
        "v_mfma_f32_16x16x32_bf16 v[28:31], a[84:87], v[176:179], v[28:31]\n\t"
        "v_mfma_f32_16x16x32_bf16 v[24:27], a[128:131], v[180:183], v[24:27]\n\t"
        "v_mfma_f32_16x16x32_bf16 v[28:31], a[132:135], v[180:183], v[28:31]\n\t"
        "buffer_store_dwordx4 v[128:131], %[xo1], s[60:63], s66 offen offset:1024 nt\n\t"
        "v_mfma_f32_16x16x32_bf16 v[24:27], a[176:179], v[184:187], v[24:27]\n\t"
        "v_cvt_pk_bf16_f32 v40, v32, v33\n\t"
        "v_add_u32 v196, 122880, v212\n\t"
        "v_mfma_f32_16x16x32_bf16 v[28:31], a[180:183], v[184:187], v[28:31]\n\t"
        "v_cvt_pk_bf16_f32 v41, v34, v35\n\t"
        "v_add_u32 v197, 122880, v213\n\t"
        "v_mfma_f32_16x16x32_bf16 v[24:27], a[224:227], v[188:191], v[24:27]\n\t"
        "v_cvt_pk_bf16_f32 v42, v36, v37\n\t"
        "s_memrealtime s[74:75]\n\t"
        "v_mfma_f32_16x16x32_bf16 v[28:31], a[228:231], v[188:191], v[28:31]\n\t"
        "ds_write_b64 v200, v[40:41] offset:128\n\t"
        "v_mfma_f32_16x16x32_bf16 v[24:27], v[108:111], v[192:195], v[24:27]\n\t"
        "v_cvt_pk_bf16_f32 v43, v38, v39\n\t"
        "v_mfma_f32_16x16x32_bf16 v[28:31], v[112:115], v[192:195], v[28:31]\n\t"
        "ds_write_b64 v201, v[42:43] offset:128\n\t"
        "s_waitcnt lgkmcnt(0)\n\t"
        "s_sub_u32 s71, s74, s72\n\t"
        "s_cmp_gt_u32 s71, 1017\n\t"
        "s_cselect_b64 vcc, -1, 0\n\t"
        "s_cbranch_vccnz .Lsk5_59\n\t"
        "s_nop 15\n\t"
        "s_nop 15\n\t"
        "s_nop 1\n\t"
        ".Lsk5_59:\n\t"
        "v_mfma_f32_16x16x32_bf16 v[32:35], a[40:43], v[172:175], v[84:87]\n\t"
        "v_mfma_f32_16x16x32_bf16 v[36:39], a[44:47], v[172:175], v[88:91]\n\t"
        "ds_read_b128 v[172:175], v196 offset:0\n\t"
        "v_mfma_f32_16x16x32_bf16 v[32:35], a[88:91], v[176:179], v[32:35]\n\t"
        "v_mfma_f32_16x16x32_bf16 v[36:39], a[92:95], v[176:179], v[36:39]\n\t"
        "ds_read_b128 v[176:179], v197 offset:0\n\t"
        "v_mfma_f32_16x16x32_bf16 v[32:35], a[136:139], v[180:183], v[32:35]\n\t"
        "v_mfma_f32_16x16x32_bf16 v[36:39], a[140:143], v[180:183], v[36:39]\n\t"
        "ds_read_b128 v[180:183], v196 offset:128\n\t"
        "v_mfma_f32_16x16x32_bf16 v[32:35], a[184:187], v[184:187], v[32:35]\n\t"
        "v_cvt_pk_bf16_f32 v40, v24, v25\n\t"
        "v_mfma_f32_16x16x32_bf16 v[36:39], a[188:191], v[184:187], v[36:39]\n\t"
        "v_cvt_pk_bf16_f32 v41, v26, v27\n\t"
        "ds_read_b128 v[184:187], v197 offset:128\n\t"
        "v_mfma_f32_16x16x32_bf16 v[32:35], a[232:235], v[188:191], v[32:35]\n\t"
        "v_cvt_pk_bf16_f32 v42, v28, v29\n\t"
        "v_mfma_f32_16x16x32_bf16 v[36:39], a[236:239], v[188:191], v[36:39]\n\t"
        "ds_write_b64 v198, v[40:41] offset:256\n\t"
        "ds_read_b128 v[188:191], v196 offset:256\n\t"
        "v_mfma_f32_16x16x32_bf16 v[32:35], v[116:119], v[192:195], v[32:35]\n\t"
        "v_cvt_pk_bf16_f32 v43, v30, v31\n\t"
        "v_mfma_f32_16x16x32_bf16 v[36:39], v[120:123], v[192:195], v[36:39]\n\t"
        "ds_write_b64 v199, v[42:43] offset:256\n\t"
        "ds_read_b128 v[192:195], v197 offset:256\n\t"
        "s_cbranch_vccnz .Lsk5_71\n\t"
        "s_nop 15\n\t"
        "s_nop 15\n\t"
        "s_nop 1\n\t"
        ".Lsk5_71:\n\t"
        "s_waitcnt lgkmcnt(7)\n\t"
        "v_mfma_f32_16x16x32_bf16 v[24:27], a[0:3], v[172:175], v[44:47]\n\t"
        "v_mfma_f32_16x16x32_bf16 v[28:31], a[4:7], v[172:175], v[48:51]\n\t"
        "s_waitcnt lgkmcnt(6)\n\t"
        "v_mfma_f32_16x16x32_bf16 v[24:27], a[48:51], v[176:179], v[24:27]\n\t"
        "v_add_u32 v202, 73728, v214\n\t"
        "v_mfma_f32_16x16x32_bf16 v[28:31], a[52:55], v[176:179], v[28:31]\n\t"
        "buffer_store_dwordx4 v[132:135], %[xo2], s[60:63], s66 offen offset:2048 nt\n\t"
        "s_waitcnt lgkmcnt(5)\n\t"
        "v_mfma_f32_16x16x32_bf16 v[24:27], a[96:99], v[180:183], v[24:27]\n\t"
        "v_mfma_f32_16x16x32_bf16 v[28:31], a[100:103], v[180:183], v[28:31]\n\t"
        "s_waitcnt lgkmcnt(4)\n\t"
        "v_mfma_f32_16x16x32_bf16 v[24:27], a[144:147], v[184:187], v[24:27]\n\t"
        "v_cvt_pk_bf16_f32 v40, v32, v33\n\t"
        "v_mfma_f32_16x16x32_bf16 v[28:31], a[148:151], v[184:187], v[28:31]\n\t"
        "v_cvt_pk_bf16_f32 v41, v34, v35\n\t"
        "s_waitcnt lgkmcnt(2)\n\t"
        "v_mfma_f32_16x16x32_bf16 v[24:27], a[192:195], v[188:191], v[24:27]\n\t"
        "v_cvt_pk_bf16_f32 v42, v36, v37\n\t"
        "v_mfma_f32_16x16x32_bf16 v[28:31], a[196:199], v[188:191], v[28:31]\n\t"
        "ds_write_b64 v200, v[40:41] offset:256\n\t"
        "s_waitcnt lgkmcnt(1)\n\t"
        "v_mfma_f32_16x16x32_bf16 v[24:27], a[240:243], v[192:195], v[24:27]\n\t"
        "v_cvt_pk_bf16_f32 v43, v38, v39\n\t"
        "v_mfma_f32_16x16x32_bf16 v[28:31], a[244:247], v[192:195], v[28:31]\n\t"
        "ds_write_b64 v201, v[42:43] offset:256\n\t"
        "s_cbranch_vccnz .Lsk6_11\n\t"
        "s_nop 15\n\t"
        "s_nop 5\n\t"
        ".Lsk6_11:\n\t"
        "v_mfma_f32_16x16x32_bf16 v[32:35], a[8:11], v[172:175], v[52:55]\n\t"
        "ds_read_b128 v[148:151], v202 offset:0\n\t"
        "v_mfma_f32_16x16x32_bf16 v[36:39], a[12:15], v[172:175], v[56:59]\n\t"
        "ds_read_b128 v[152:155], v202 offset:1024\n\t"
        "v_mfma_f32_16x16x32_bf16 v[32:35], a[56:59], v[176:179], v[32:35]\n\t"
        "v_add_u32 v198, 122880, v208\n\t"
        "ds_read_b128 v[156:159], v202 offset:2048\n\t"
        "v_mfma_f32_16x16x32_bf16 v[36:39], a[60:63], v[176:179], v[36:39]\n\t"
        "v_add_u32 v199, 122880, v209\n\t"
        "ds_read_b128 v[160:163], v202 offset:3072\n\t"
        "v_mfma_f32_16x16x32_bf16 v[32:35], a[104:107], v[180:183], v[32:35]\n\t"
        "ds_read_b128 v[164:167], v202 offset:4096\n\t"
        "v_mfma_f32_16x16x32_bf16 v[36:39], a[108:111], v[180:183], v[36:39]\n\t"
        "ds_read_b128 v[168:171], v202 offset:5120\n\t"
        "v_mfma_f32_16x16x32_bf16 v[32:35], a[152:155], v[184:187], v[32:35]\n\t"
        "v_cvt_pk_bf16_f32 v40, v24, v25\n\t"
        "v_add_u32 v200, 122880, v210\n\t"
        "v_mfma_f32_16x16x32_bf16 v[36:39], a[156:159], v[184:187], v[36:39]\n\t"
        "v_cvt_pk_bf16_f32 v41, v26, v27\n\t"
        "v_add_u32 v201, 122880, v211\n\t"
        "v_mfma_f32_16x16x32_bf16 v[32:35], a[200:203], v[188:191], v[32:35]\n\t"
        "v_cvt_pk_bf16_f32 v42, v28, v29\n\t"
        "v_mfma_f32_16x16x32_bf16 v[36:39], a[204:207], v[188:191], v[36:39]\n\t"
        "ds_write_b64 v198, v[40:41] offset:0\n\t"
        "v_mfma_f32_16x16x32_bf16 v[32:35], a[248:251], v[192:195], v[32:35]\n\t"
        "v_cvt_pk_bf16_f32 v43, v30, v31\n\t"
        "v_mfma_f32_16x16x32_bf16 v[36:39], a[252:255], v[192:195], v[36:39]\n\t"
        "ds_write_b64 v199, v[42:43] offset:0\n\t"
        "s_cbranch_vccnz .Lsk6_23\n\t"
        "s_nop 15\n\t"
        "s_nop 5\n\t"
        ".Lsk6_23:\n\t"
        "v_mfma_f32_16x16x32_bf16 v[24:27], a[16:19], v[172:175], v[60:63]\n\t"
        "v_mfma_f32_16x16x32_bf16 v[28:31], a[20:23], v[172:175], v[64:67]\n\t"
        "v_mfma_f32_16x16x32_bf16 v[24:27], a[64:67], v[176:179], v[24:27]\n\t"
        "buffer_store_dwordx4 v[136:139], %[xo3], s[60:63], s66 offen nt\n\t"
        "v_mfma_f32_16x16x32_bf16 v[28:31], a[68:71], v[176:179], v[28:31]\n\t"
        "v_mfma_f32_16x16x32_bf16 v[24:27], a[112:115], v[180:183], v[24:27]\n\t"
        "v_mfma_f32_16x16x32_bf16 v[28:31], a[116:119], v[180:183], v[28:31]\n\t"
        "buffer_store_dwordx4 v[140:143], %[xo4], s[60:63], s66 offen offset:1024 nt\n\t"
        "v_mfma_f32_16x16x32_bf16 v[24:27], a[160:163], v[184:187], v[24:27]\n\t"
        "v_cvt_pk_bf16_f32 v40, v32, v33\n\t"
        "v_mfma_f32_16x16x32_bf16 v[28:31], a[164:167], v[184:187], v[28:31]\n\t"
        "v_cvt_pk_bf16_f32 v41, v34, v35\n\t"
        "s_memrealtime s[74:75]\n\t"
        "v_mfma_f32_16x16x32_bf16 v[24:27], a[208:211], v[188:191], v[24:27]\n\t"
        "v_cvt_pk_bf16_f32 v42, v36, v37\n\t"
        "v_mfma_f32_16x16x32_bf16 v[28:31], a[212:215], v[188:191], v[28:31]\n\t"
        "ds_write_b64 v200, v[40:41] offset:0\n\t"
        "v_mfma_f32_16x16x32_bf16 v[24:27], v[92:95], v[192:195], v[24:27]\n\t"
        "v_cvt_pk_bf16_f32 v43, v38, v39\n\t"
        "v_mfma_f32_16x16x32_bf16 v[28:31], v[96:99], v[192:195], v[28:31]\n\t"
        "ds_write_b64 v201, v[42:43] offset:0\n\t"
        "s_waitcnt lgkmcnt(0)\n\t"
        "s_sub_u32 s71, s74, s72\n\t"
        "s_cmp_gt_u32 s71, 1083\n\t"
        "s_cselect_b64 vcc, -1, 0\n\t"
        "s_cbranch_vccnz .Lsk6_35\n\t"
        "s_nop 15\n\t"
        "s_nop 5\n\t"
        ".Lsk6_35:\n\t"
        "v_mfma_f32_16x16x32_bf16 v[32:35], a[24:27], v[172:175], v[68:71]\n\t"
        "v_mfma_f32_16x16x32_bf16 v[36:39], a[28:31], v[172:175], v[72:75]\n\t"
        "v_mfma_f32_16x16x32_bf16 v[32:35], a[72:75], v[176:179], v[32:35]\n\t"
        "buffer_store_dwordx4 v[144:147], %[xo5], s[60:63], s66 offen offset:2048 nt\n\t"
        "s_add_u32 s69, s69, 2039808\n\t"
        "s_sub_u32 s71, s69, %[ts]\n\t"
        "s_cselect_b32 s69, s69, s71\n\t"
        "s_add_u32 s70, s70, %[ts]\n\t"
        "s_add_u32 s66, s70, s69\n\t"
        "v_mfma_f32_16x16x32_bf16 v[36:39], a[76:79], v[176:179], v[36:39]\n\t"
        "v_mfma_f32_16x16x32_bf16 v[32:35], a[120:123], v[180:183], v[32:35]\n\t"
        "v_mfma_f32_16x16x32_bf16 v[36:39], a[124:127], v[180:183], v[36:39]\n\t"
        "buffer_store_dwordx4 v[148:151], %[xo0], s[60:63], s66 offen nt\n\t"
        "v_mfma_f32_16x16x32_bf16 v[32:35], a[168:171], v[184:187], v[32:35]\n\t"
        "v_cvt_pk_bf16_f32 v40, v24, v25\n\t"
        "v_mfma_f32_16x16x32_bf16 v[36:39], a[172:175], v[184:187], v[36:39]\n\t"
        "v_cvt_pk_bf16_f32 v41, v26, v27\n\t"
        "v_mfma_f32_16x16x32_bf16 v[32:35], a[216:219], v[188:191], v[32:35]\n\t"
        "v_cvt_pk_bf16_f32 v42, v28, v29\n\t"
        "v_mfma_f32_16x16x32_bf16 v[36:39], a[220:223], v[188:191], v[36:39]\n\t"
        "ds_write_b64 v198, v[40:41] offset:128\n\t"
        "v_mfma_f32_16x16x32_bf16 v[32:35], v[100:103], v[192:195], v[32:35]\n\t"
        "v_cvt_pk_bf16_f32 v43, v30, v31\n\t"
        "v_mfma_f32_16x16x32_bf16 v[36:39], v[104:107], v[192:195], v[36:39]\n\t"
        "ds_write_b64 v199, v[42:43] offset:128\n\t"
        "s_cbranch_vccnz .Lsk6_47\n\t"
        "s_nop 15\n\t"
        "s_nop 5\n\t"
        ".Lsk6_47:\n\t"
        "v_mfma_f32_16x16x32_bf16 v[24:27], a[32:35], v[172:175], v[76:79]\n\t"
        "v_mfma_f32_16x16x32_bf16 v[28:31], a[36:39], v[172:175], v[80:83]\n\t"
        "v_mfma_f32_16x16x32_bf16 v[24:27], a[80:83], v[176:179], v[24:27]\n\t"
        "buffer_store_dwordx4 v[152:155], %[xo1], s[60:63], s66 offen offset:1024 nt\n\t"
        "v_mfma_f32_16x16x32_bf16 v[28:31], a[84:87], v[176:179], v[28:31]\n\t"
        "v_mfma_f32_16x16x32_bf16 v[24:27], a[128:131], v[180:183], v[24:27]\n\t"
        "v_mfma_f32_16x16x32_bf16 v[28:31], a[132:135], v[180:183], v[28:31]\n\t"
        "buffer_store_dwordx4 v[156:159], %[xo2], s[60:63], s66 offen offset:2048 nt\n\t"
        "v_mfma_f32_16x16x32_bf16 v[24:27], a[176:179], v[184:187], v[24:27]\n\t"
        "v_cvt_pk_bf16_f32 v40, v32, v33\n\t"
        "v_add_u32 v196, 24576, v212\n\t"
        "v_mfma_f32_16x16x32_bf16 v[28:31], a[180:183], v[184:187], v[28:31]\n\t"
        "v_cvt_pk_bf16_f32 v41, v34, v35\n\t"
        "v_add_u32 v197, 24576, v213\n\t"
        "v_mfma_f32_16x16x32_bf16 v[24:27], a[224:227], v[188:191], v[24:27]\n\t"
        "v_cvt_pk_bf16_f32 v42, v36, v37\n\t"
        "s_memrealtime s[74:75]\n\t"
        "v_mfma_f32_16x16x32_bf16 v[28:31], a[228:231], v[188:191], v[28:31]\n\t"
        "ds_write_b64 v200, v[40:41] offset:128\n\t"
        "v_mfma_f32_16x16x32_bf16 v[24:27], v[108:111], v[192:195], v[24:27]\n\t"
        "v_cvt_pk_bf16_f32 v43, v38, v39\n\t"
        "v_mfma_f32_16x16x32_bf16 v[28:31], v[112:115], v[192:195], v[28:31]\n\t"
        "ds_write_b64 v201, v[42:43] offset:128\n\t"
        "s_waitcnt lgkmcnt(0)\n\t"
        "s_sub_u32 s71, s74, s72\n\t"
        "s_cmp_gt_u32 s71, 1117\n\t"
        "s_cselect_b64 vcc, -1, 0\n\t"
        "s_cbranch_vccnz .Lsk6_59\n\t"
        "s_nop 15\n\t"
        "s_nop 5\n\t"
        ".Lsk6_59:\n\t"
        "v_mfma_f32_16x16x32_bf16 v[32:35], a[40:43], v[172:175], v[84:87]\n\t"
        "v_mfma_f32_16x16x32_bf16 v[36:39], a[44:47], v[172:175], v[88:91]\n\t"
        "ds_read_b128 v[172:175], v196 offset:0\n\t"
        "v_mfma_f32_16x16x32_bf16 v[32:35], a[88:91], v[176:179], v[32:35]\n\t"
        "v_mfma_f32_16x16x32_bf16 v[36:39], a[92:95], v[176:179], v[36:39]\n\t"
        "ds_read_b128 v[176:179], v197 offset:0\n\t"
        "v_mfma_f32_16x16x32_bf16 v[32:35], a[136:139], v[180:183], v[32:35]\n\t"
        "v_mfma_f32_16x16x32_bf16 v[36:39], a[140:143], v[180:183], v[36:39]\n\t"
        "ds_read_b128 v[180:183], v196 offset:128\n\t"
        "v_mfma_f32_16x16x32_bf16 v[32:35], a[184:187], v[184:187], v[32:35]\n\t"
        "v_cvt_pk_bf16_f32 v40, v24, v25\n\t"
        "v_mfma_f32_16x16x32_bf16 v[36:39], a[188:191], v[184:187], v[36:39]\n\t"
        "v_cvt_pk_bf16_f32 v41, v26, v27\n\t"
        "ds_read_b128 v[184:187], v197 offset:128\n\t"
        "v_mfma_f32_16x16x32_bf16 v[32:35], a[232:235], v[188:191], v[32:35]\n\t"
        "v_cvt_pk_bf16_f32 v42, v28, v29\n\t"
        "v_mfma_f32_16x16x32_bf16 v[36:39], a[236:239], v[188:191], v[36:39]\n\t"
        "ds_write_b64 v198, v[40:41] offset:256\n\t"
        "ds_read_b128 v[188:191], v196 offset:256\n\t"
        "v_mfma_f32_16x16x32_bf16 v[32:35], v[116:119], v[192:195], v[32:35]\n\t"
        "v_cvt_pk_bf16_f32 v43, v30, v31\n\t"
        "v_mfma_f32_16x16x32_bf16 v[36:39], v[120:123], v[192:195], v[36:39]\n\t"
        "ds_write_b64 v199, v[42:43] offset:256\n\t"
        "ds_read_b128 v[192:195], v197 offset:256\n\t"
        "s_cbranch_vccnz .Lsk6_71\n\t"
        "s_nop 15\n\t"
        "s_nop 5\n\t"
        ".Lsk6_71:\n\t"
        "s_waitcnt lgkmcnt(7)\n\t"
        "v_mfma_f32_16x16x32_bf16 v[24:27], a[0:3], v[172:175], v[44:47]\n\t"
        "v_mfma_f32_16x16x32_bf16 v[28:31], a[4:7], v[172:175], v[48:51]\n\t"
        "s_waitcnt lgkmcnt(6)\n\t"
        "v_mfma_f32_16x16x32_bf16 v[24:27], a[48:51], v[176:179], v[24:27]\n\t"
        "v_add_u32 v202, 98304, v214\n\t"
        "v_mfma_f32_16x16x32_bf16 v[28:31], a[52:55], v[176:179], v[28:31]\n\t"
        "buffer_store_dwordx4 v[160:163], %[xo3], s[60:63], s66 offen nt\n\t"
        "s_waitcnt lgkmcnt(5)\n\t"
        "v_mfma_f32_16x16x32_bf16 v[24:27], a[96:99], v[180:183], v[24:27]\n\t"
        "v_mfma_f32_16x16x32_bf16 v[28:31], a[100:103], v[180:183], v[28:31]\n\t"
        "s_waitcnt lgkmcnt(4)\n\t"
        "v_mfma_f32_16x16x32_bf16 v[24:27], a[144:147], v[184:187], v[24:27]\n\t"
        "v_cvt_pk_bf16_f32 v40, v32, v33\n\t"
        "v_mfma_f32_16x16x32_bf16 v[28:31], a[148:151], v[184:187], v[28:31]\n\t"
        "v_cvt_pk_bf16_f32 v41, v34, v35\n\t"
        "s_waitcnt lgkmcnt(2)\n\t"
        "v_mfma_f32_16x16x32_bf16 v[24:27], a[192:195], v[188:191], v[24:27]\n\t"
        "v_cvt_pk_bf16_f32 v42, v36, v37\n\t"
        "v_mfma_f32_16x16x32_bf16 v[28:31], a[196:199], v[188:191], v[28:31]\n\t"
        "ds_write_b64 v200, v[40:41] offset:256\n\t"
        "s_waitcnt lgkmcnt(1)\n\t"
        "v_mfma_f32_16x16x32_bf16 v[24:27], a[240:243], v[192:195], v[24:27]\n\t"
        "v_cvt_pk_bf16_f32 v43, v38, v39\n\t"
        "v_mfma_f32_16x16x32_bf16 v[28:31], a[244:247], v[192:195], v[28:31]\n\t"
        "ds_write_b64 v201, v[42:43] offset:256\n\t"
        "s_cbranch_vccnz .Lsk7_11\n\t"
        "s_nop 15\n\t"
        "s_nop 5\n\t"
        ".Lsk7_11:\n\t"
        "v_mfma_f32_16x16x32_bf16 v[32:35], a[8:11], v[172:175], v[52:55]\n\t"
        "ds_read_b128 v[0:3], v202 offset:0\n\t"
        "v_mfma_f32_16x16x32_bf16 v[36:39], a[12:15], v[172:175], v[56:59]\n\t"
        "ds_read_b128 v[4:7], v202 offset:1024\n\t"
        "v_mfma_f32_16x16x32_bf16 v[32:35], a[56:59], v[176:179], v[32:35]\n\t"
        "v_add_u32 v198, 24576, v208\n\t"
        "ds_read_b128 v[8:11], v202 offset:2048\n\t"
        "v_mfma_f32_16x16x32_bf16 v[36:39], a[60:63], v[176:179], v[36:39]\n\t"
        "v_add_u32 v199, 24576, v209\n\t"
        "ds_read_b128 v[12:15], v202 offset:3072\n\t"
        "v_mfma_f32_16x16x32_bf16 v[32:35], a[104:107], v[180:183], v[32:35]\n\t"
        "ds_read_b128 v[16:19], v202 offset:4096\n\t"
        "v_mfma_f32_16x16x32_bf16 v[36:39], a[108:111], v[180:183], v[36:39]\n\t"
        "ds_read_b128 v[20:23], v202 offset:5120\n\t"
        "v_mfma_f32_16x16x32_bf16 v[32:35], a[152:155], v[184:187], v[32:35]\n\t"
        "v_cvt_pk_bf16_f32 v40, v24, v25\n\t"
        "v_add_u32 v200, 24576, v210\n\t"
        "v_mfma_f32_16x16x32_bf16 v[36:39], a[156:159], v[184:187], v[36:39]\n\t"
        "v_cvt_pk_bf16_f32 v41, v26, v27\n\t"
        "v_add_u32 v201, 24576, v211\n\t"
        "v_mfma_f32_16x16x32_bf16 v[32:35], a[200:203], v[188:191], v[32:35]\n\t"
        "v_cvt_pk_bf16_f32 v42, v28, v29\n\t"
        "v_mfma_f32_16x16x32_bf16 v[36:39], a[204:207], v[188:191], v[36:39]\n\t"
        "ds_write_b64 v198, v[40:41] offset:0\n\t"
        "v_mfma_f32_16x16x32_bf16 v[32:35], a[248:251], v[192:195], v[32:35]\n\t"
        "v_cvt_pk_bf16_f32 v43, v30, v31\n\t"
        "v_mfma_f32_16x16x32_bf16 v[36:39], a[252:255], v[192:195], v[36:39]\n\t"
        "ds_write_b64 v199, v[42:43] offset:0\n\t"
        "s_cbranch_vccnz .Lsk7_23\n\t"
        "s_nop 15\n\t"
        "s_nop 5\n\t"
        ".Lsk7_23:\n\t"
        "v_mfma_f32_16x16x32_bf16 v[24:27], a[16:19], v[172:175], v[60:63]\n\t"
        "v_mfma_f32_16x16x32_bf16 v[28:31], a[20:23], v[172:175], v[64:67]\n\t"
        "v_mfma_f32_16x16x32_bf16 v[24:27], a[64:67], v[176:179], v[24:27]\n\t"
        "buffer_store_dwordx4 v[164:167], %[xo4], s[60:63], s66 offen offset:1024 nt\n\t"
        "v_mfma_f32_16x16x32_bf16 v[28:31], a[68:71], v[176:179], v[28:31]\n\t"
        "v_mfma_f32_16x16x32_bf16 v[24:27], a[112:115], v[180:183], v[24:27]\n\t"
        "v_mfma_f32_16x16x32_bf16 v[28:31], a[116:119], v[180:183], v[28:31]\n\t"
        "buffer_store_dwordx4 v[168:171], %[xo5], s[60:63], s66 offen offset:2048 nt\n\t"
        "s_add_u32 s69, s69, 2039808\n\t"
        "s_sub_u32 s71, s69, %[ts]\n\t"
        "s_cselect_b32 s69, s69, s71\n\t"
        "s_add_u32 s70, s70, %[ts]\n\t"
        "s_add_u32 s66, s70, s69\n\t"
        "v_mfma_f32_16x16x32_bf16 v[24:27], a[160:163], v[184:187], v[24:27]\n\t"
        "v_cvt_pk_bf16_f32 v40, v32, v33\n\t"
        "v_mfma_f32_16x16x32_bf16 v[28:31], a[164:167], v[184:187], v[28:31]\n\t"
        "v_cvt_pk_bf16_f32 v41, v34, v35\n\t"
        "s_memrealtime s[74:75]\n\t"
        "v_mfma_f32_16x16x32_bf16 v[24:27], a[208:211], v[188:191], v[24:27]\n\t"
        "v_cvt_pk_bf16_f32 v42, v36, v37\n\t"
        "v_mfma_f32_16x16x32_bf16 v[28:31], a[212:215], v[188:191], v[28:31]\n\t"
        "ds_write_b64 v200, v[40:41] offset:0\n\t"
        "v_mfma_f32_16x16x32_bf16 v[24:27], v[92:95], v[192:195], v[24:27]\n\t"
        "v_cvt_pk_bf16_f32 v43, v38, v39\n\t"
        "v_mfma_f32_16x16x32_bf16 v[28:31], v[96:99], v[192:195], v[28:31]\n\t"
        "ds_write_b64 v201, v[42:43] offset:0\n\t"
        "s_waitcnt lgkmcnt(0)\n\t"
        "s_sub_u32 s71, s74, s72\n\t"
        "s_cmp_gt_u32 s71, 1181\n\t"
        "s_cselect_b64 vcc, -1, 0\n\t"
        "s_cbranch_vccnz .Lsk7_35\n\t"
        "s_nop 15\n\t"
        "s_nop 5\n\t"
        ".Lsk7_35:\n\t"
        "v_mfma_f32_16x16x32_bf16 v[32:35], a[24:27], v[172:175], v[68:71]\n\t"
        "v_mfma_f32_16x16x32_bf16 v[36:39], a[28:31], v[172:175], v[72:75]\n\t"
        "v_mfma_f32_16x16x32_bf16 v[32:35], a[72:75], v[176:179], v[32:35]\n\t"
        "buffer_store_dwordx4 v[0:3], %[xo0], s[60:63], s66 offen nt\n\t"
        "v_mfma_f32_16x16x32_bf16 v[36:39], a[76:79], v[176:179], v[36:39]\n\t"
        "v_mfma_f32_16x16x32_bf16 v[32:35], a[120:123], v[180:183], v[32:35]\n\t"
        "v_mfma_f32_16x16x32_bf16 v[36:39], a[124:127], v[180:183], v[36:39]\n\t"
        "buffer_store_dwordx4 v[4:7], %[xo1], s[60:63], s66 offen offset:1024 nt\n\t"
        "v_mfma_f32_16x16x32_bf16 v[32:35], a[168:171], v[184:187], v[32:35]\n\t"
        "v_cvt_pk_bf16_f32 v40, v24, v25\n\t"
        "v_mfma_f32_16x16x32_bf16 v[36:39], a[172:175], v[184:187], v[36:39]\n\t"
        "v_cvt_pk_bf16_f32 v41, v26, v27\n\t"
        "v_mfma_f32_16x16x32_bf16 v[32:35], a[216:219], v[188:191], v[32:35]\n\t"
        "v_cvt_pk_bf16_f32 v42, v28, v29\n\t"
        "v_mfma_f32_16x16x32_bf16 v[36:39], a[220:223], v[188:191], v[36:39]\n\t"
        "ds_write_b64 v198, v[40:41] offset:128\n\t"
        "v_mfma_f32_16x16x32_bf16 v[32:35], v[100:103], v[192:195], v[32:35]\n\t"
        "v_cvt_pk_bf16_f32 v43, v30, v31\n\t"
        "v_mfma_f32_16x16x32_bf16 v[36:39], v[104:107], v[192:195], v[36:39]\n\t"
        "ds_write_b64 v199, v[42:43] offset:128\n\t"
        "s_cbranch_vccnz .Lsk7_47\n\t"
        "s_nop 15\n\t"
        "s_nop 5\n\t"
        ".Lsk7_47:\n\t"
        "v_mfma_f32_16x16x32_bf16 v[24:27], a[32:35], v[172:175], v[76:79]\n\t"
        "v_mfma_f32_16x16x32_bf16 v[28:31], a[36:39], v[172:175], v[80:83]\n\t"
        "v_mfma_f32_16x16x32_bf16 v[24:27], a[80:83], v[176:179], v[24:27]\n\t"
        "buffer_store_dwordx4 v[8:11], %[xo2], s[60:63], s66 offen offset:2048 nt\n\t"
        "v_mfma_f32_16x16x32_bf16 v[28:31], a[84:87], v[176:179], v[28:31]\n\t"
        "v_mfma_f32_16x16x32_bf16 v[24:27], a[128:131], v[180:183], v[24:27]\n\t"
        "v_mfma_f32_16x16x32_bf16 v[28:31], a[132:135], v[180:183], v[28:31]\n\t"
        "buffer_store_dwordx4 v[12:15], %[xo3], s[60:63], s66 offen nt\n\t"
        "v_mfma_f32_16x16x32_bf16 v[24:27], a[176:179], v[184:187], v[24:27]\n\t"
        "v_cvt_pk_bf16_f32 v40, v32, v33\n\t"
        "v_add_u32 v196, 49152, v212\n\t"
        "v_mfma_f32_16x16x32_bf16 v[28:31], a[180:183], v[184:187], v[28:31]\n\t"
        "v_cvt_pk_bf16_f32 v41, v34, v35\n\t"
        "v_add_u32 v197, 49152, v213\n\t"
        "v_mfma_f32_16x16x32_bf16 v[24:27], a[224:227], v[188:191], v[24:27]\n\t"
        "v_cvt_pk_bf16_f32 v42, v36, v37\n\t"
        "s_memrealtime s[74:75]\n\t"
        "v_mfma_f32_16x16x32_bf16 v[28:31], a[228:231], v[188:191], v[28:31]\n\t"
        "ds_write_b64 v200, v[40:41] offset:128\n\t"
        "v_mfma_f32_16x16x32_bf16 v[24:27], v[108:111], v[192:195], v[24:27]\n\t"
        "v_cvt_pk_bf16_f32 v43, v38, v39\n\t"
        "v_mfma_f32_16x16x32_bf16 v[28:31], v[112:115], v[192:195], v[28:31]\n\t"
        "ds_write_b64 v201, v[42:43] offset:128\n\t"
        "s_waitcnt lgkmcnt(0)\n\t"
        "s_sub_u32 s71, s74, s72\n\t"
        "s_cmp_gt_u32 s71, 1215\n\t"
        "s_cselect_b64 vcc, -1, 0\n\t"
        "s_cbranch_vccnz .Lsk7_59\n\t"
        "s_nop 15\n\t"
        "s_nop 5\n\t"
        ".Lsk7_59:\n\t"
        "v_mfma_f32_16x16x32_bf16 v[32:35], a[40:43], v[172:175], v[84:87]\n\t"
        "v_mfma_f32_16x16x32_bf16 v[36:39], a[44:47], v[172:175], v[88:91]\n\t"
        "ds_read_b128 v[172:175], v196 offset:0\n\t"
        "v_mfma_f32_16x16x32_bf16 v[32:35], a[88:91], v[176:179], v[32:35]\n\t"
        "v_mfma_f32_16x16x32_bf16 v[36:39], a[92:95], v[176:179], v[36:39]\n\t"
        "ds_read_b128 v[176:179], v197 offset:0\n\t"
        "v_mfma_f32_16x16x32_bf16 v[32:35], a[136:139], v[180:183], v[32:35]\n\t"
        "v_mfma_f32_16x16x32_bf16 v[36:39], a[140:143], v[180:183], v[36:39]\n\t"
        "ds_read_b128 v[180:183], v196 offset:128\n\t"
        "v_mfma_f32_16x16x32_bf16 v[32:35], a[184:187], v[184:187], v[32:35]\n\t"
        "v_cvt_pk_bf16_f32 v40, v24, v25\n\t"
        "v_mfma_f32_16x16x32_bf16 v[36:39], a[188:191], v[184:187], v[36:39]\n\t"
        "v_cvt_pk_bf16_f32 v41, v26, v27\n\t"
        "ds_read_b128 v[184:187], v197 offset:128\n\t"
        "v_mfma_f32_16x16x32_bf16 v[32:35], a[232:235], v[188:191], v[32:35]\n\t"
        "v_cvt_pk_bf16_f32 v42, v28, v29\n\t"
        "v_mfma_f32_16x16x32_bf16 v[36:39], a[236:239], v[188:191], v[36:39]\n\t"
        "ds_write_b64 v198, v[40:41] offset:256\n\t"
        "ds_read_b128 v[188:191], v196 offset:256\n\t"
        "v_mfma_f32_16x16x32_bf16 v[32:35], v[116:119], v[192:195], v[32:35]\n\t"
        "v_cvt_pk_bf16_f32 v43, v30, v31\n\t"
        "v_mfma_f32_16x16x32_bf16 v[36:39], v[120:123], v[192:195], v[36:39]\n\t"
        "ds_write_b64 v199, v[42:43] offset:256\n\t"
        "ds_read_b128 v[192:195], v197 offset:256\n\t"
        "s_cbranch_vccnz .Lsk7_71\n\t"
        "s_nop 15\n\t"
        "s_nop 5\n\t"
        ".Lsk7_71:\n\t"
        "s_waitcnt lgkmcnt(7)\n\t"
        "v_mfma_f32_16x16x32_bf16 v[24:27], a[0:3], v[172:175], v[44:47]\n\t"
        "v_mfma_f32_16x16x32_bf16 v[28:31], a[4:7], v[172:175], v[48:51]\n\t"
        "s_waitcnt lgkmcnt(6)\n\t"
        "v_mfma_f32_16x16x32_bf16 v[24:27], a[48:51], v[176:179], v[24:27]\n\t"
        "v_add_u32 v202, 122880, v214\n\t"
        "v_mfma_f32_16x16x32_bf16 v[28:31], a[52:55], v[176:179], v[28:31]\n\t"
        "buffer_store_dwordx4 v[16:19], %[xo4], s[60:63], s66 offen offset:1024 nt\n\t"
        "s_waitcnt lgkmcnt(5)\n\t"
        "v_mfma_f32_16x16x32_bf16 v[24:27], a[96:99], v[180:183], v[24:27]\n\t"
        "v_mfma_f32_16x16x32_bf16 v[28:31], a[100:103], v[180:183], v[28:31]\n\t"
        "s_waitcnt lgkmcnt(4)\n\t"
        "v_mfma_f32_16x16x32_bf16 v[24:27], a[144:147], v[184:187], v[24:27]\n\t"
        "v_cvt_pk_bf16_f32 v40, v32, v33\n\t"
        "v_mfma_f32_16x16x32_bf16 v[28:31], a[148:151], v[184:187], v[28:31]\n\t"
        "v_cvt_pk_bf16_f32 v41, v34, v35\n\t"
        "s_waitcnt lgkmcnt(2)\n\t"
        "v_mfma_f32_16x16x32_bf16 v[24:27], a[192:195], v[188:191], v[24:27]\n\t"
        "v_cvt_pk_bf16_f32 v42, v36, v37\n\t"
        "v_mfma_f32_16x16x32_bf16 v[28:31], a[196:199], v[188:191], v[28:31]\n\t"
        "ds_write_b64 v200, v[40:41] offset:256\n\t"
        "s_waitcnt lgkmcnt(1)\n\t"
        "v_mfma_f32_16x16x32_bf16 v[24:27], a[240:243], v[192:195], v[24:27]\n\t"
        "v_cvt_pk_bf16_f32 v43, v38, v39\n\t"
        "v_mfma_f32_16x16x32_bf16 v[28:31], a[244:247], v[192:195], v[28:31]\n\t"
        "ds_write_b64 v201, v[42:43] offset:256\n\t"
        "s_cbranch_vccnz .Lsk8_11\n\t"
        "s_nop 15\n\t"
        "s_nop 5\n\t"
        ".Lsk8_11:\n\t"
        "v_mfma_f32_16x16x32_bf16 v[32:35], a[8:11], v[172:175], v[52:55]\n\t"
        "ds_read_b128 v[124:127], v202 offset:0\n\t"
        "v_mfma_f32_16x16x32_bf16 v[36:39], a[12:15], v[172:175], v[56:59]\n\t"
        "ds_read_b128 v[128:131], v202 offset:1024\n\t"
        "v_mfma_f32_16x16x32_bf16 v[32:35], a[56:59], v[176:179], v[32:35]\n\t"
        "v_add_u32 v198, 49152, v208\n\t"
        "ds_read_b128 v[132:135], v202 offset:2048\n\t"
        "v_mfma_f32_16x16x32_bf16 v[36:39], a[60:63], v[176:179], v[36:39]\n\t"
        "v_add_u32 v199, 49152, v209\n\t"
        "ds_read_b128 v[136:139], v202 offset:3072\n\t"
        "v_mfma_f32_16x16x32_bf16 v[32:35], a[104:107], v[180:183], v[32:35]\n\t"
        "ds_read_b128 v[140:143], v202 offset:4096\n\t"
        "v_mfma_f32_16x16x32_bf16 v[36:39], a[108:111], v[180:183], v[36:39]\n\t"
        "ds_read_b128 v[144:147], v202 offset:5120\n\t"
        "v_mfma_f32_16x16x32_bf16 v[32:35], a[152:155], v[184:187], v[32:35]\n\t"
        "v_cvt_pk_bf16_f32 v40, v24, v25\n\t"
        "v_add_u32 v200, 49152, v210\n\t"
        "v_mfma_f32_16x16x32_bf16 v[36:39], a[156:159], v[184:187], v[36:39]\n\t"
        "v_cvt_pk_bf16_f32 v41, v26, v27\n\t"
        "v_add_u32 v201, 49152, v211\n\t"
        "v_mfma_f32_16x16x32_bf16 v[32:35], a[200:203], v[188:191], v[32:35]\n\t"
        "v_cvt_pk_bf16_f32 v42, v28, v29\n\t"
        "v_mfma_f32_16x16x32_bf16 v[36:39], a[204:207], v[188:191], v[36:39]\n\t"
        "ds_write_b64 v198, v[40:41] offset:0\n\t"
        "v_mfma_f32_16x16x32_bf16 v[32:35], a[248:251], v[192:195], v[32:35]\n\t"
        "v_cvt_pk_bf16_f32 v43, v30, v31\n\t"
        "v_mfma_f32_16x16x32_bf16 v[36:39], a[252:255], v[192:195], v[36:39]\n\t"
        "ds_write_b64 v199, v[42:43] offset:0\n\t"
        "s_cbranch_vccnz .Lsk8_23\n\t"
        "s_nop 15\n\t"
        "s_nop 5\n\t"
        ".Lsk8_23:\n\t"
        "v_mfma_f32_16x16x32_bf16 v[24:27], a[16:19], v[172:175], v[60:63]\n\t"
        "v_mfma_f32_16x16x32_bf16 v[28:31], a[20:23], v[172:175], v[64:67]\n\t"
        "v_mfma_f32_16x16x32_bf16 v[24:27], a[64:67], v[176:179], v[24:27]\n\t"
        "buffer_store_dwordx4 v[20:23], %[xo5], s[60:63], s66 offen offset:2048 nt\n\t"
        "s_add_u32 s69, s69, 2039808\n\t"
        "s_sub_u32 s71, s69, %[ts]\n\t"
        "s_cselect_b32 s69, s69, s71\n\t"
        "s_add_u32 s70, s70, %[ts]\n\t"
        "s_add_u32 s66, s70, s69\n\t"
        "v_mfma_f32_16x16x32_bf16 v[28:31], a[68:71], v[176:179], v[28:31]\n\t"
        "v_mfma_f32_16x16x32_bf16 v[24:27], a[112:115], v[180:183], v[24:27]\n\t"
        "v_mfma_f32_16x16x32_bf16 v[28:31], a[116:119], v[180:183], v[28:31]\n\t"
        "s_waitcnt lgkmcnt(2)\n\t"
        "buffer_store_dwordx4 v[124:127], %[xo0], s[60:63], s66 offen nt\n\t"
        "v_mfma_f32_16x16x32_bf16 v[24:27], a[160:163], v[184:187], v[24:27]\n\t"
        "v_cvt_pk_bf16_f32 v40, v32, v33\n\t"
        "v_mfma_f32_16x16x32_bf16 v[28:31], a[164:167], v[184:187], v[28:31]\n\t"
        "v_cvt_pk_bf16_f32 v41, v34, v35\n\t"
        "s_memrealtime s[74:75]\n\t"
        "v_mfma_f32_16x16x32_bf16 v[24:27], a[208:211], v[188:191], v[24:27]\n\t"
        "v_cvt_pk_bf16_f32 v42, v36, v37\n\t"
        "v_mfma_f32_16x16x32_bf16 v[28:31], a[212:215], v[188:191], v[28:31]\n\t"
        "ds_write_b64 v200, v[40:41] offset:0\n\t"
        "v_mfma_f32_16x16x32_bf16 v[24:27], v[92:95], v[192:195], v[24:27]\n\t"
        "v_cvt_pk_bf16_f32 v43, v38, v39\n\t"
        "v_mfma_f32_16x16x32_bf16 v[28:31], v[96:99], v[192:195], v[28:31]\n\t"
        "ds_write_b64 v201, v[42:43] offset:0\n\t"
        "s_waitcnt lgkmcnt(0)\n\t"
        "s_sub_u32 s71, s74, s72\n\t"
        "s_cmp_gt_u32 s71, 1279\n\t"
        "s_cselect_b64 vcc, -1, 0\n\t"
        "s_cbranch_vccnz .Lsk8_35\n\t"
        "s_nop 15\n\t"
        "s_nop 5\n\t"
        ".Lsk8_35:\n\t"
        "v_mfma_f32_16x16x32_bf16 v[32:35], a[24:27], v[172:175], v[68:71]\n\t"
        "v_mfma_f32_16x16x32_bf16 v[36:39], a[28:31], v[172:175], v[72:75]\n\t"
        "v_mfma_f32_16x16x32_bf16 v[32:35], a[72:75], v[176:179], v[32:35]\n\t"
        "buffer_store_dwordx4 v[128:131], %[xo1], s[60:63], s66 offen offset:1024 nt\n\t"
        "v_mfma_f32_16x16x32_bf16 v[36:39], a[76:79], v[176:179], v[36:39]\n\t"
        "v_mfma_f32_16x16x32_bf16 v[32:35], a[120:123], v[180:183], v[32:35]\n\t"
        "v_mfma_f32_16x16x32_bf16 v[36:39], a[124:127], v[180:183], v[36:39]\n\t"
        "buffer_store_dwordx4 v[132:135], %[xo2], s[60:63], s66 offen offset:2048 nt\n\t"
        "v_mfma_f32_16x16x32_bf16 v[32:35], a[168:171], v[184:187], v[32:35]\n\t"
        "v_cvt_pk_bf16_f32 v40, v24, v25\n\t"
        "v_mfma_f32_16x16x32_bf16 v[36:39], a[172:175], v[184:187], v[36:39]\n\t"
        "v_cvt_pk_bf16_f32 v41, v26, v27\n\t"
        "v_mfma_f32_16x16x32_bf16 v[32:35], a[216:219], v[188:191], v[32:35]\n\t"
        "v_cvt_pk_bf16_f32 v42, v28, v29\n\t"
        "v_mfma_f32_16x16x32_bf16 v[36:39], a[220:223], v[188:191], v[36:39]\n\t"
        "ds_write_b64 v198, v[40:41] offset:128\n\t"
        "v_mfma_f32_16x16x32_bf16 v[32:35], v[100:103], v[192:195], v[32:35]\n\t"
        "v_cvt_pk_bf16_f32 v43, v30, v31\n\t"
        "v_mfma_f32_16x16x32_bf16 v[36:39], v[104:107], v[192:195], v[36:39]\n\t"
        "ds_write_b64 v199, v[42:43] offset:128\n\t"
        "s_cbranch_vccnz .Lsk8_47\n\t"
        "s_nop 15\n\t"
        "s_nop 5\n\t"
        ".Lsk8_47:\n\t"
        "v_mfma_f32_16x16x32_bf16 v[24:27], a[32:35], v[172:175], v[76:79]\n\t"
        "v_mfma_f32_16x16x32_bf16 v[28:31], a[36:39], v[172:175], v[80:83]\n\t"
        "v_mfma_f32_16x16x32_bf16 v[24:27], a[80:83], v[176:179], v[24:27]\n\t"
        "buffer_store_dwordx4 v[136:139], %[xo3], s[60:63], s66 offen nt\n\t"
        "v_mfma_f32_16x16x32_bf16 v[28:31], a[84:87], v[176:179], v[28:31]\n\t"
        "v_mfma_f32_16x16x32_bf16 v[24:27], a[128:131], v[180:183], v[24:27]\n\t"
        "v_mfma_f32_16x16x32_bf16 v[28:31], a[132:135], v[180:183], v[28:31]\n\t"
        "buffer_store_dwordx4 v[140:143], %[xo4], s[60:63], s66 offen offset:1024 nt\n\t"
        "v_mfma_f32_16x16x32_bf16 v[24:27], a[176:179], v[184:187], v[24:27]\n\t"
        "v_cvt_pk_bf16_f32 v40, v32, v33\n\t"
        "v_mfma_f32_16x16x32_bf16 v[28:31], a[180:183], v[184:187], v[28:31]\n\t"
        "v_cvt_pk_bf16_f32 v41, v34, v35\n\t"
        "v_mfma_f32_16x16x32_bf16 v[24:27], a[224:227], v[188:191], v[24:27]\n\t"
        "v_cvt_pk_bf16_f32 v42, v36, v37\n\t"
        "v_mfma_f32_16x16x32_bf16 v[28:31], a[228:231], v[188:191], v[28:31]\n\t"
        "ds_write_b64 v200, v[40:41] offset:128\n\t"
        "v_mfma_f32_16x16x32_bf16 v[24:27], v[108:111], v[192:195], v[24:27]\n\t"
        "v_cvt_pk_bf16_f32 v43, v38, v39\n\t"
        "v_mfma_f32_16x16x32_bf16 v[28:31], v[112:115], v[192:195], v[28:31]\n\t"
        "ds_write_b64 v201, v[42:43] offset:128\n\t"
        "s_cbranch_vccnz .Lsk8_59\n\t"
        "s_nop 15\n\t"
        "s_nop 5\n\t"
        ".Lsk8_59:\n\t"
        "v_mfma_f32_16x16x32_bf16 v[32:35], a[40:43], v[172:175], v[84:87]\n\t"
        "v_mfma_f32_16x16x32_bf16 v[36:39], a[44:47], v[172:175], v[88:91]\n\t"
        "v_mfma_f32_16x16x32_bf16 v[32:35], a[88:91], v[176:179], v[32:35]\n\t"
        "v_mfma_f32_16x16x32_bf16 v[36:39], a[92:95], v[176:179], v[36:39]\n\t"
        "v_mfma_f32_16x16x32_bf16 v[32:35], a[136:139], v[180:183], v[32:35]\n\t"
        "v_mfma_f32_16x16x32_bf16 v[36:39], a[140:143], v[180:183], v[36:39]\n\t"
        "v_mfma_f32_16x16x32_bf16 v[32:35], a[184:187], v[184:187], v[32:35]\n\t"
        "v_cvt_pk_bf16_f32 v40, v24, v25\n\t"
        "v_mfma_f32_16x16x32_bf16 v[36:39], a[188:191], v[184:187], v[36:39]\n\t"
        "v_cvt_pk_bf16_f32 v41, v26, v27\n\t"
        "v_mfma_f32_16x16x32_bf16 v[32:35], a[232:235], v[188:191], v[32:35]\n\t"
        "v_cvt_pk_bf16_f32 v42, v28, v29\n\t"
        "v_mfma_f32_16x16x32_bf16 v[36:39], a[236:239], v[188:191], v[36:39]\n\t"
        "ds_write_b64 v198, v[40:41] offset:256\n\t"
        "v_mfma_f32_16x16x32_bf16 v[32:35], v[116:119], v[192:195], v[32:35]\n\t"
        "v_cvt_pk_bf16_f32 v43, v30, v31\n\t"
        "v_mfma_f32_16x16x32_bf16 v[36:39], v[120:123], v[192:195], v[36:39]\n\t"
        "ds_write_b64 v199, v[42:43] offset:256\n\t"
        "s_cbranch_vccnz .Lsk8_71\n\t"
        "s_nop 15\n\t"
        "s_nop 5\n\t"
        ".Lsk8_71:\n\t"
        "s_nop 7\n\t"
        "v_cvt_pk_bf16_f32 v40, v32, v33\n\t"
        "v_cvt_pk_bf16_f32 v41, v34, v35\n\t"
        "v_cvt_pk_bf16_f32 v42, v36, v37\n\t"
        "v_cvt_pk_bf16_f32 v43, v38, v39\n\t"
        "ds_write_b64 v200, v[40:41] offset:256\n\t"
        "ds_write_b64 v201, v[42:43] offset:256\n\t"
        "v_add_u32 v202, 24576, v214\n\t"
        "ds_read_b128 v[148:151], v202 offset:0\n\t"
        "ds_read_b128 v[152:155], v202 offset:1024\n\t"
        "ds_read_b128 v[156:159], v202 offset:2048\n\t"
        "ds_read_b128 v[160:163], v202 offset:3072\n\t"
        "ds_read_b128 v[164:167], v202 offset:4096\n\t"
        "ds_read_b128 v[168:171], v202 offset:5120\n\t"
        "v_add_u32 v202, 49152, v214\n\t"
        "ds_read_b128 v[0:3], v202 offset:0\n\t"
        "ds_read_b128 v[4:7], v202 offset:1024\n\t"
        "ds_read_b128 v[8:11], v202 offset:2048\n\t"
        "ds_read_b128 v[12:15], v202 offset:3072\n\t"
        "ds_read_b128 v[16:19], v202 offset:4096\n\t"
        "ds_read_b128 v[20:23], v202 offset:5120\n\t"
        "buffer_store_dwordx4 v[144:147], %[xo5], s[60:63], s66 offen offset:2048 nt\n\t"
        "s_add_u32 s69, s69, 2039808\n\t"
        "s_sub_u32 s71, s69, %[ts]\n\t"
        "s_cselect_b32 s69, s69, s71\n\t"
        "s_add_u32 s70, s70, %[ts]\n\t"
        "s_add_u32 s66, s70, s69\n\t"
        "s_waitcnt lgkmcnt(6)\n\t"
        "buffer_store_dwordx4 v[148:151], %[xo0], s[60:63], s66 offen nt\n\t"
        "buffer_store_dwordx4 v[152:155], %[xo1], s[60:63], s66 offen offset:1024 nt\n\t"
        "buffer_store_dwordx4 v[156:159], %[xo2], s[60:63], s66 offen offset:2048 nt\n\t"
        "buffer_store_dwordx4 v[160:163], %[xo3], s[60:63], s66 offen nt\n\t"
        "buffer_store_dwordx4 v[164:167], %[xo4], s[60:63], s66 offen offset:1024 nt\n\t"
        "buffer_store_dwordx4 v[168:171], %[xo5], s[60:63], s66 offen offset:2048 nt\n\t"
        "s_add_u32 s69, s69, 2039808\n\t"
        "s_sub_u32 s71, s69, %[ts]\n\t"
        "s_cselect_b32 s69, s69, s71\n\t"
        "s_add_u32 s70, s70, %[ts]\n\t"
        "s_add_u32 s66, s70, s69\n\t"
        "s_waitcnt lgkmcnt(0)\n\t"
        "buffer_store_dwordx4 v[0:3], %[xo0], s[60:63], s66 offen nt\n\t"
        "buffer_store_dwordx4 v[4:7], %[xo1], s[60:63], s66 offen offset:1024 nt\n\t"
        "buffer_store_dwordx4 v[8:11], %[xo2], s[60:63], s66 offen offset:2048 nt\n\t"
        "buffer_store_dwordx4 v[12:15], %[xo3], s[60:63], s66 offen nt\n\t"
        "buffer_store_dwordx4 v[16:19], %[xo4], s[60:63], s66 offen offset:1024 nt\n\t"
        "buffer_store_dwordx4 v[20:23], %[xo5], s[60:63], s66 offen offset:2048 nt\n\t"
        "s_endpgm\n\t"
        ".fill 64, 4, 0xbf800000\n\t"
        ".Lconv_end:\n\t"
        :
        : [xo0] "v"(xo[0]), [xo1] "v"(xo[1]), [xo2] "v"(xo[2]), [xo3] "v"(xo[3]), [xo4] "v"(xo[4]), [xo5] "v"(xo[5]),
          [wo0] "v"(wo[0]), [wo1] "v"(wo[1]), [wo2] "v"(wo[2]), [wo3] "v"(wo[3]), [wo4] "v"(wo[4]), [wo5] "v"(wo[5]),
          [lane] "v"(lane), [wbase] "s"(lbase + W_OFF), [stg] "s"(lbase + STG_OFF + wid * 2048),
          [rx] "s"(rx), [rw] "s"(rw), [sl0] "s"(sl0), [ws] "s"(wid * SLOT), [t0] "s"(t0), [ts] "s"(tstride),
          [nb] "s"(nbytes), [olo] "s"(olo), [ohi] "s"(ohi)
        : "v0", "v1", "v2", "v3", "v4", "v5", "v6", "v7", "v8", "v9", "v10", "v11", "v12", "v13", "v14", "v15", "v16",
          "v17", "v18", "v19", "v20", "v21", "v22", "v23", "v24", "v25", "v26", "v27", "v28", "v29", "v30", "v31",
          "v32", "v33", "v34", "v35", "v36", "v37", "v38", "v39", "v40", "v41", "v42", "v43", "v44", "v45", "v46",
          "v47", "v48", "v49", "v50", "v51", "v52", "v53", "v54", "v55", "v56", "v57", "v58", "v59", "v60", "v61",
          "v62", "v63", "v64", "v65", "v66", "v67", "v68", "v69", "v70", "v71", "v72", "v73", "v74", "v75", "v76",
          "v77", "v78", "v79", "v80", "v81", "v82", "v83", "v84", "v85", "v86", "v87", "v88", "v89", "v90", "v91",
          "v92", "v93", "v94", "v95", "v96", "v97", "v98", "v99", "v100", "v101", "v102", "v103", "v104", "v105",
          "v106", "v107", "v108", "v109", "v110", "v111", "v112", "v113", "v114", "v115", "v116", "v117", "v118",
          "v119", "v120", "v121", "v122", "v123", "v124", "v125", "v126", "v127", "v128", "v129", "v130", "v131",
          "v132", "v133", "v134", "v135", "v136", "v137", "v138", "v139", "v140", "v141", "v142", "v143", "v144",
          "v145", "v146", "v147", "v148", "v149", "v150", "v151", "v152", "v153", "v154", "v155", "v156", "v157",
          "v158", "v159", "v160", "v161", "v162", "v163", "v164", "v165", "v166", "v167", "v168", "v169", "v170",
          "v171", "v172", "v173", "v174", "v175", "v176", "v177", "v178", "v179", "v180", "v181", "v182", "v183",
          "v184", "v185", "v186", "v187", "v188", "v189", "v190", "v191", "v192", "v193", "v194", "v195", "v196",
          "v197", "v198", "v199", "v200", "v201", "v202", "v203", "v204", "v205", "v206", "v207", "v208", "v209",
          "v210", "v211", "v212", "v213", "v214", "v215", "v216", "v217", "v218", "v219", "v220", "v221", "a0", "a1",
          "a2", "a3", "a4", "a5", "a6", "a7", "a8", "a9", "a10", "a11", "a12", "a13", "a14", "a15", "a16", "a17", "a18",
          "a19", "a20", "a21", "a22", "a23", "a24", "a25", "a26", "a27", "a28", "a29", "a30", "a31", "a32", "a33",
          "a34", "a35", "a36", "a37", "a38", "a39", "a40", "a41", "a42", "a43", "a44", "a45", "a46", "a47", "a48",
          "a49", "a50", "a51", "a52", "a53", "a54", "a55", "a56", "a57", "a58", "a59", "a60", "a61", "a62", "a63",
          "a64", "a65", "a66", "a67", "a68", "a69", "a70", "a71", "a72", "a73", "a74", "a75", "a76", "a77", "a78",
          "a79", "a80", "a81", "a82", "a83", "a84", "a85", "a86", "a87", "a88", "a89", "a90", "a91", "a92", "a93",
          "a94", "a95", "a96", "a97", "a98", "a99", "a100", "a101", "a102", "a103", "a104", "a105", "a106", "a107",
          "a108", "a109", "a110", "a111", "a112", "a113", "a114", "a115", "a116", "a117", "a118", "a119", "a120",
          "a121", "a122", "a123", "a124", "a125", "a126", "a127", "a128", "a129", "a130", "a131", "a132", "a133",
          "a134", "a135", "a136", "a137", "a138", "a139", "a140", "a141", "a142", "a143", "a144", "a145", "a146",
          "a147", "a148", "a149", "a150", "a151", "a152", "a153", "a154", "a155", "a156", "a157", "a158", "a159",
          "a160", "a161", "a162", "a163", "a164", "a165", "a166", "a167", "a168", "a169", "a170", "a171", "a172",
          "a173", "a174", "a175", "a176", "a177", "a178", "a179", "a180", "a181", "a182", "a183", "a184", "a185",
          "a186", "a187", "a188", "a189", "a190", "a191", "a192", "a193", "a194", "a195", "a196", "a197", "a198",
          "a199", "a200", "a201", "a202", "a203", "a204", "a205", "a206", "a207", "a208", "a209", "a210", "a211",
          "a212", "a213", "a214", "a215", "a216", "a217", "a218", "a219", "a220", "a221", "a222", "a223", "a224",
          "a225", "a226", "a227", "a228", "a229", "a230", "a231", "a232", "a233", "a234", "a235", "a236", "a237",
          "a238", "a239", "a240", "a241", "a242", "a243", "a244", "a245", "a246", "a247", "a248", "a249", "a250",
          "a251", "a252", "a253", "a254", "a255", "s60", "s61", "s62", "s63", "s64", "s65", "s66", "s67", "s68", "s69",
          "s70", "s71", "s72", "s73", "s74", "s75", "scc", "vcc", "memory");
}

__global__ __launch_bounds__(C) void conv1x1_generic(const unsigned short* __restrict__ x, const unsigned short* __restrict__ w,
                                                     const unsigned short* __restrict__ bias, __hip_bfloat16* __restrict__ out) {
    const size_t m = blockIdx.x;
    const int n = threadIdx.x;
    float acc = __builtin_bit_cast(float, (unsigned)bias[n] << 16);
    for (int k = 0; k < C; ++k)
        acc += __builtin_bit_cast(float, (unsigned)x[m * C + k] << 16) * __builtin_bit_cast(float, (unsigned)w[k * C + n] << 16);
    out[m * C + n] = __float2bfloat16(acc);
}

extern "C" void kernel_launch(void* const* d_in, const int* in_sizes, int n_in,
                              void* d_out, int out_size, void* d_ws, size_t ws_size,
                              hipStream_t stream) {
    const int M = in_sizes[0] / C;
    const int rows_per_wg = 16 * NW * TPW;
    const int grid = M / rows_per_wg;
    if (M % rows_per_wg == 0 && grid > 83 && (size_t)M * ROWB < (size_t)OOR) {
        conv1x1_kernel<<<grid, NW * 64, 0, stream>>>(d_in[0], d_in[1], d_in[2], d_out, (unsigned)(M * ROWB),
                                                     (unsigned)(grid * NW * SLOT));
    } else if (M > 0) {
        conv1x1_generic<<<M, C, 0, stream>>>((const unsigned short*)d_in[0], (const unsigned short*)d_in[1],
                                             (const unsigned short*)d_in[2], (__hip_bfloat16*)d_out);
    }
}
```
